```python
import torch

def _softmax(x, axis=-1):
    return torch.softmax(x, dim=axis)

def _gelu(x):
    return torch.nn.functional.gelu(x, approximate='tanh')

def _silu(x):
    return torch.nn.functional.silu(x)

def _sigmoid(x):
    return torch.sigmoid(x)

def _layernorm(x, axis=-1, eps=1e-05):
    m = x.mean(dim=axis, keepdim=True)
    v = x.var(dim=axis, keepdim=True, unbiased=False)
    return (x - m) / torch.sqrt(v + eps)

def _scatter_add(tgt, idx, src, axis=0):
    return tgt.clone().index_add_(axis, idx, src)

def _segment_sum(src, seg, n):
    out = torch.zeros((n,) + src.shape[1:], dtype=src.dtype)
    return out.index_add_(0, seg, src)

def _pad(x, pw):
    flat = tuple((v for pair in reversed(pw) for v in pair))
    return torch.nn.functional.pad(x, flat)
kernel_name = 'concat_heads__memory_aug_recurrent_mem_tr__linear_kernel_hedgehog__prompt'

def setup_inputs():

    _w = lambda a: (lambda t: t.to(torch.bfloat16) if t.is_floating_point() else t)(torch.as_tensor(a))

    def _A_setup():
        B, S, Dm = (1, 2048, 448)
        H, Dh = (16, 256)
        M, seg = (8, 32)
        seg = min(seg, S)
        S = S // seg * seg
        return {'x': _w((lambda *s: torch.randn(*s, dtype=torch.bfloat16) * 0.05)(B, S, Dm)), 'mem0': _w((lambda *s: torch.randn(*s, dtype=torch.bfloat16) * 0.8)(B, M, Dm)), 'Wq': _w((lambda *s: torch.randn(*s, dtype=torch.bfloat16) * 1.6)(Dm, H, Dh) * Dm ** (-0.5)), 'Wk': _w((lambda *s: torch.randn(*s, dtype=torch.bfloat16) * 1.6)(Dm, H, Dh) * Dm ** (-0.5)), 'Wv': _w((lambda *s: torch.randn(*s, dtype=torch.bfloat16) * 0.8)(Dm, H, Dh) * Dm ** (-0.5)), 'Wo': _w((lambda *s: torch.randn(*s, dtype=torch.bfloat16) * 0.8)(H, Dh, Dm) * (H * Dh) ** (-0.5)), 'seg_len': seg}
    _A_in = _A_setup()

    def _B_setup():
        B, S, D, H = (1, 2048, 448, 16)
        Dh = D // H
        return {'x': _w((lambda *s: torch.randn(*s, dtype=torch.bfloat16) * 0.4)(B, S, D)), 'wqkv': _w((lambda *s: torch.randn(*s, dtype=torch.bfloat16) * 0.4)(D, 3 * D)), 'w_phi': _w((lambda *s: torch.randn(*s, dtype=torch.bfloat16) * 0.4)(H, Dh, Dh) * Dh ** (-0.5)), 'wo': _w((lambda *s: torch.randn(*s, dtype=torch.bfloat16) * 0.05)(D, D))}
    _B_in = _B_setup()
    return {'A_x': _A_in['x'], 'A_mem0': _A_in['mem0'], 'A_Wq': _A_in['Wq'], 'A_Wk': _A_in['Wk'], 'A_Wv': _A_in['Wv'], 'A_Wo': _A_in['Wo'], 'A_seg_len': _A_in['seg_len'], 'B_x': _B_in['x'], 'B_wqkv': _B_in['wqkv'], 'B_w_phi': _B_in['w_phi'], 'B_wo': _B_in['wo']}

def reference(A_x, A_mem0, A_Wq, A_Wk, A_Wv, A_Wo, A_seg_len, B_x, B_wqkv, B_w_phi, B_wo):

    def _A_ref(x, mem0, Wq, Wk, Wv, Wo, seg_len):
        B, S, Dm = x.shape
        H, Dh = (Wq.shape[1], Wq.shape[2])
        M = mem0.shape[1]
        scale = Dh ** (-0.5)
        n_seg = S // seg_len
        L = M + seg_len
        idx = torch.arange(L)
        mask = (idx[:, None] < M) | (idx[:, None] >= idx[None, :])
        mem = mem0
        outs = []
        for s in range(n_seg):
            xs = x[:, s * seg_len:(s + 1) * seg_len, :].clone()
            h = torch.concatenate([mem, xs], axis=1)
            q = torch.einsum('btd,dhe->bhte', h, Wq)
            k = torch.einsum('btd,dhe->bhte', h, Wk)
            v = torch.einsum('btd,dhe->bhte', h, Wv)
            scores = torch.einsum('bhqe,bhke->bhqk', q, k) * scale
            scores = torch.where(mask[None, None], scores, -1000000000.0)
            p = _softmax(scores, axis=-1)
            o = torch.einsum('bhqk,bhke->bhqe', p, v)
            o = torch.einsum('bhte,hed->btd', o, Wo)
            mem = o[:, :M, :].clone()
            outs.append(o[:, M:, :])
        return torch.concatenate(outs, axis=1)

    def _B_ref(x, wqkv, w_phi, wo):
        B, S, D = x.shape
        H = 16
        Dh = D // H
        qkv = (lambda x, *d: x.permute(*d))((x @ wqkv).reshape(B, S, 3, H, Dh), 2, 0, 3, 1, 4)
        q, k, v = (qkv[0], qkv[1], qkv[2])
        pq = torch.einsum('bhsd,hdf->bhsf', q, w_phi)
        pk = torch.einsum('bhsd,hdf->bhsf', k, w_phi)
        phi_q = torch.concatenate([_softmax(pq, axis=-1), _softmax(-pq, axis=-1)], axis=-1)
        phi_k = torch.concatenate([_softmax(pk, axis=-1), _softmax(-pk, axis=-1)], axis=-1)
        kv = torch.einsum('bhsf,bhsd->bhfd', phi_k, v)
        z = torch.einsum('bhsf->bhf', phi_k)
        num = torch.einsum('bhqf,bhfd->bhqd', phi_q, kv)
        den = torch.einsum('bhqf,bhf->bhq', phi_q, z)[..., None] + 1e-06
        o = num / den
        return (lambda x, *d: x.permute(*d))(o, 0, 2, 1, 3).reshape(B, S, D) @ wo
    a = _A_ref(A_x, A_mem0, A_Wq, A_Wk, A_Wv, A_Wo, A_seg_len)
    b = _B_ref(B_x, B_wqkv, B_w_phi, B_wo)
    return torch.concatenate([a, b], axis=-1)
```

```cpp
#include <hip/hip_runtime.h>
#include <hip/hip_bf16.h>
#include <stdint.h>
#include <type_traits>

typedef __attribute__((ext_vector_type(8))) __bf16 bf8v;
typedef __attribute__((ext_vector_type(4))) short s4v;
typedef __attribute__((ext_vector_type(2))) __bf16 bf2v;
typedef __attribute__((ext_vector_type(4))) float f32x4;
typedef __attribute__((ext_vector_type(2))) float f32x2;
typedef __attribute__((ext_vector_type(4))) unsigned u32x4;
typedef __attribute__((ext_vector_type(2))) unsigned u32x2;
typedef unsigned short u16;
#define LAS __attribute__((address_space(3)))
#define GAS __attribute__((address_space(1)))

constexpr int DM = 448, NH = 16, EH = 256, NSEG = 64, KK = 14;
constexpr float LOG2E = 1.4426950408889634f;
constexpr unsigned SENT = 0x7F817F81u;

constexpr size_t MB = 1u << 20;
constexpr size_t WS_CTRL = 0;
constexpr size_t WS_WA = 1 * MB;
constexpr size_t WS_WB = 8 * MB;
constexpr size_t WS_MG = 16 * MB;
constexpr size_t WS_MW = 24 * MB;
constexpr size_t WS_PX = 40 * MB;
constexpr size_t WS_PM = 43 * MB;
constexpr size_t WS_XF = 44 * MB;
constexpr size_t WS_BXF = 46 * MB;
constexpr size_t WS_BW = 48 * MB;
constexpr size_t WS_BWO = 50 * MB;
constexpr size_t WS_KVP = 51 * MB;
constexpr size_t WS_PHQ = 53 * MB;
constexpr size_t WS_HO = 64 * MB;
constexpr unsigned R_GF = 16u << 20;
constexpr unsigned R_UF = 80u << 20;
#define FAR_T 1
__host__ __device__ constexpr unsigned gmap(unsigned o) { return ((o & ~8191u) << 1) | (o & 8191u); }
__device__ __forceinline__ unsigned seg_base(int seg, unsigned parity) { return ((unsigned)seg << 20) + (((((unsigned)seg >> 1) ^ parity ^ FAR_T) & 1u) << 13); }

constexpr unsigned HO_COPY = 7 * 16384;
constexpr unsigned HO_STEP = 2 * HO_COPY;
constexpr unsigned HO_WC = 2u << 20;
constexpr int HO_SPC = 9;
constexpr unsigned HO_BYTES = 144u << 20;
__host__ __device__ constexpr unsigned ho_step_base(int s) { return (unsigned)(s / HO_SPC) * (2u << 20) + (unsigned)(s % HO_SPC) * HO_STEP; }
constexpr unsigned HO_M = 0;
constexpr unsigned HO_SMM = 16384;
constexpr unsigned HO_SX = 5 * 16384;

__device__ __forceinline__ f32x4 mfma32(u32x4 a, u32x4 b, f32x4 c) {
    return __builtin_amdgcn_mfma_f32_16x16x32_bf16(__builtin_bit_cast(bf8v, a), __builtin_bit_cast(bf8v, b), c, 0, 0, 0);
}
__device__ __forceinline__ unsigned pk2(float a, float b) {
    f32x2 v = {a, b};
    return __builtin_bit_cast(unsigned, __builtin_convertvector(v, bf2v));
}
__device__ __forceinline__ u32x4 pk8(f32x4 a, f32x4 b) { return u32x4{pk2(a[0], a[1]), pk2(a[2], a[3]), pk2(b[0], b[1]), pk2(b[2], b[3])}; }
#define WT_AUX 16
__device__ __forceinline__ void st_wt(unsigned char* ws, size_t off, u32x4 v) {
    auto r = __builtin_amdgcn_make_buffer_rsrc(ws, 0, 64u << 20, 0x00020000);
    __builtin_amdgcn_raw_buffer_store_b128(v, r, (unsigned)off, 0, WT_AUX);
}
__device__ __forceinline__ void glds16(const void* g, unsigned lds_base) {
    unsigned sv;
    asm volatile("s_mov_b32 %0, m0\n\ts_mov_b32 m0, %2\n\ts_nop 0\n\tglobal_load_lds_dwordx4 %1, off\n\ts_mov_b32 m0, %0" : "=&s"(sv) : "v"(g), "s"(lds_base) : "memory");
}
#define LDS_BAR() asm volatile("s_waitcnt lgkmcnt(0)\n\ts_barrier" ::: "memory")
#define SBAR() __builtin_amdgcn_sched_barrier(0)
__device__ __forceinline__ unsigned xcc_id() { return (unsigned)__builtin_amdgcn_s_getreg(20 | (3 << 11)) & 0xfu; }
__device__ __forceinline__ int perm32(int rho, int t) { return 8 * (rho >> 2) + 4 * t + (rho & 3); }

struct Params {
    const u16* x; const u16* mem0; const u16* wq; const u16* wk; const u16* wv; const u16* wo;
    const u16* bx; const u16* bwqkv; const u16* bwphi; const u16* bwo;
    u16* out; unsigned char* ws;
};
__device__ __forceinline__ unsigned char* ho_base(unsigned char* ws) { return (unsigned char*)(((uintptr_t)ws + WS_HO + (2u << 20) - 1) & ~(uintptr_t)((2u << 20) - 1)); }

#define CAL_N 14
#define C1_T 80
#define C2_T 88
constexpr unsigned HO_PAR = (2u << 20) - 32768;
constexpr unsigned HO_CNT = 1u << 20;

template <bool isA>
__device__ __forceinline__ void prep_ab(const Params& p, unsigned char* smem, int b, int u, int wave, int tid) {
    const int lane = tid & 63, rho = lane & 15, q = lane >> 4;
    const int h = (b & 7) * 2 + (u >> 3), ch = (u >> 1) & 1, rh = u & 1;
    const unsigned lds0 = (unsigned)(uintptr_t)smem;
    const u16* src[7];
#pragma unroll
    for (int i = 0; i < 7; ++i) {
        const int R = ((wave & 3) * 7 + i) * 8 + (lane >> 3), tl = R >> 4, row = 32 * (tl >> 1) + perm32(R & 15, tl & 1), c = (lane & 7) ^ ((R >> 1) & 7);
        if (wave < 4) src[i] = (isA ? p.wk : p.wv) + (size_t)(224 * rh + row) * 4096 + h * 256 + 8 * c;
        else if (isA) src[i] = p.wq + (size_t)(224 * ch + row) * 4096 + h * 256 + 8 * c;
        else { const int U = ((wave & 3) * 7 + i) * 64 + lane; src[i] = p.wo + ((size_t)h * 256 + U / 28) * DM + 224 * ch + 8 * (U % 28); }
    }
    const int sstep = (isA || wave < 4) ? 64 : 64 * DM;
    auto stage = [&](int st) {
        const unsigned dst = lds0 + (st & 1) * 57344 + wave * 7168;
#pragma unroll
        for (int i = 0; i < 7; ++i) glds16(src[i] + sstep * st, (unsigned)__builtin_amdgcn_readfirstlane(dst + i * 1024));
    };
    f32x4 acc[14][2];
#pragma unroll
    for (int c = 0; c < 14; ++c) { acc[c][0] = f32x4{0, 0, 0, 0}; acc[c][1] = f32x4{0, 0, 0, 0}; }
    const int pr = wave < 7 ? wave : 6;
    stage(0); stage(1);
#pragma unroll
    for (int st = 0; st < 4; ++st) {
        if (st < 3) asm volatile("s_waitcnt vmcnt(7)\n\ts_barrier" ::: "memory"); else asm volatile("s_waitcnt vmcnt(0)\n\ts_barrier" ::: "memory");
        const LAS unsigned char* P = (const LAS unsigned char*)smem + (st & 1) * 57344;
        const LAS unsigned char* Q = P + 28672;
        const int sw = (rho >> 1) & 7;
        if (wave < 7) {
#pragma unroll
            for (int k2 = 0; k2 < 2; ++k2) {
                const int co = ((4 * k2 + q) ^ sw) * 16;
                u32x4 a0 = *(const LAS u32x4*)(P + ((2 * pr) * 16 + rho) * 128 + co), a1 = *(const LAS u32x4*)(P + ((2 * pr + 1) * 16 + rho) * 128 + co);
#pragma unroll
                for (int c = 0; c < 14; ++c) {
                    u32x4 bf;
                    if (isA) bf = *(const LAS u32x4*)(Q + (c * 16 + rho) * 128 + co);
                    else {
                        const LAS unsigned char* base = Q + (32 * k2 + 8 * q + (rho >> 2)) * 448 + (16 * c + 4 * (rho & 3)) * 2;
                        u32x2 l2 = __builtin_bit_cast(u32x2, __builtin_amdgcn_ds_read_tr16_b64_v4i16((LAS s4v*)base));
                        u32x2 h2 = __builtin_bit_cast(u32x2, __builtin_amdgcn_ds_read_tr16_b64_v4i16((LAS s4v*)(base + 4 * 448)));
                        bf = u32x4{l2[0], l2[1], h2[0], h2[1]};
                    }
                    acc[c][0] = mfma32(a0, bf, acc[c][0]); acc[c][1] = mfma32(a1, bf, acc[c][1]);
                }
                SBAR();
            }
        }
        if (st < 2) { LDS_BAR(); stage(st + 2); }
    }
    const float sc = isA ? 0.0625f * LOG2E : 1.0f;
    const size_t obase = (isA ? WS_WA : WS_WB) + ((size_t)(h * 28 + 14 * ch) * KK + 7 * rh + pr) * 1024 + lane * 16;
    if (wave < 7) {
#pragma unroll
        for (int c = 0; c < 14; ++c) st_wt(p.ws, obase + c * KK * 1024, pk8(acc[c][0] * sc, acc[c][1] * sc));
    }
}

__global__ void __launch_bounds__(512, 2) prep_kernel(Params p) {
    extern __shared__ __attribute__((aligned(16))) unsigned char smem[];
    const int b = blockIdx.x, tid = threadIdx.x, lane = tid & 63;
    const int wave = __builtin_amdgcn_readfirstlane(tid >> 6);
    const int rho = lane & 15, q = lane >> 4;
    auto rs = __builtin_amdgcn_make_buffer_rsrc(ho_base(p.ws), 0, HO_BYTES, 0x00020000);

    const int u = b >> 3;
    if (u < 16) {
        if (u & 4) prep_ab<true>(p, smem, b, u, wave, tid); else prep_ab<false>(p, smem, b, u, wave, tid);
    } else if (wave < 4) {
        const int m = (u - 16) * 8 + (b & 7);
        unsigned* ctrl = (unsigned*)(p.ws + WS_CTRL);
        if (m >= 120) {
            const int c = m - 120;
            GAS unsigned* a = (GAS unsigned*)(ho_base(p.ws) + (size_t)c * (2u << 20) + (2u << 20) - 16384 + (wave & 1) * 8192 + (wave >> 1) * 4096);
            unsigned best = 0xffffffffu;
            if (lane == 0)
                for (int it = 0; it < CAL_N; ++it) {
                    unsigned v, z = 0;
                    unsigned long long c0 = __builtin_amdgcn_s_memtime();
                    asm volatile("global_atomic_add %0, %1, %2, off sc0\n\ts_waitcnt vmcnt(0)" : "=v"(v) : "v"(a), "v"(z) : "memory");
                    unsigned long long c1 = __builtin_amdgcn_s_memtime();
                    unsigned dt = (unsigned)(c1 - c0);
                    best = dt < best ? dt : best;
                }
            if (lane == 0) ((unsigned*)smem)[wave] = best;
            LDS_BAR();
            if (tid == 0) {
                const unsigned* bs = (const unsigned*)smem;
                const int d = (int)(bs[0] + bs[2]) - (int)(bs[1] + bs[3]);
                ctrl[c] = (unsigned)(xcc_id() >= 4 ? -d : d);
            }
            if (c == 0) for (int i = 16 + tid; i < 2688; i += 256) ctrl[i] = 0u;
            if (c == 1) __builtin_amdgcn_raw_buffer_store_b32((tid & 127) < 2 ? (unsigned)(C1_T | (C2_T << 16)) : SENT, rs, HO_PAR + tid * 64, 0, 16);
            if (c == 2) __builtin_amdgcn_raw_buffer_store_b32(0u, rs, HO_CNT + tid * 64, 0, 16);
        } else if (m >= 112) {
            for (int i = tid; i < 2 * DM; i += 256) {
                const int h = (m - 112) * 2 + i / DM, c = i % DM;
                u16 v[32];
#pragma unroll
                for (int e = 0; e < 32; ++e) v[e] = e < 28 ? p.bwo[(size_t)(h * 28 + e) * DM + c] : (u16)0;
#pragma unroll
                for (int qq = 0; qq < 4; ++qq) {
                    u32x4 o;
#pragma unroll
                    for (int k = 0; k < 4; ++k) o[k] = (unsigned)v[8 * qq + 2 * k] | ((unsigned)v[8 * qq + 2 * k + 1] << 16);
                    *(u32x4*)(p.ws + WS_BWO + ((size_t)(c >> 4) * 16 + h) * 1024 + (qq * 16 + (c & 15)) * 16) = o;
                }
            }
        } else {
            u32x4 xv[8];
#pragma unroll
            for (int z = 0; z < 8; ++z) {
                const int g = m * 32 + wave * 8 + z, f = g % 1792, seg = f / 28, r = f % 28, tt = r / KK, kk = r % KK;
                xv[z] = *(const u32x4*)((g < 1792 ? p.x : p.bx) + (size_t)(seg * 32 + perm32(rho, tt)) * DM + 32 * kk + 8 * q);
            }
            u32x4 ph[2], wa[4][2];
            const int which = m >> 4, hb = m & 15;
            if (m < 48) {
#pragma unroll
                for (int t = 0; t < 2; ++t) {
                    const int f = which < 2 ? perm32(rho, t) : 16 * t + rho;
                    unsigned short v[8];
#pragma unroll
                    for (int jj = 0; jj < 8; ++jj) {
                        const int e = 8 * q + jj;
                        const bool ok = e < 28 && f < 28;
                        v[jj] = which < 2 ? (ok ? p.bwphi[(hb * 28 + e) * 28 + f] : (u16)0) : ((ok && e == f) ? (u16)0x3F80 : (u16)0);
                    }
                    ph[t] = u32x4{(unsigned)v[0] | ((unsigned)v[1] << 16), (unsigned)v[2] | ((unsigned)v[3] << 16), (unsigned)v[4] | ((unsigned)v[5] << 16), (unsigned)v[6] | ((unsigned)v[7] << 16)};
                }
#pragma unroll
                for (int z = 0; z < 4; ++z) {
                    const int kk = wave + 4 * z < KK ? wave + 4 * z : wave;
#pragma unroll
                    for (int t = 0; t < 2; ++t) {
                        const u32x2* src = (const u32x2*)(p.bwqkv + (size_t)(32 * kk + perm32(rho, t)) * 1344 + which * 448 + hb * 28 + 8 * q);
                        u32x2 lo = src[0], hi = q < 3 ? src[1] : u32x2{0, 0};
                        wa[z][t] = u32x4{lo[0], lo[1], hi[0], hi[1]};
                    }
                }
            }
#pragma unroll
            for (int i = 0; i < 2; ++i) {
                const unsigned g = (unsigned)m * 2 + i, s = g / 56, w = (g % 56) * 4096 + tid * 16;
                u32x4 v = {SENT, SENT, SENT, SENT};
                if (s == 0) {
                    const unsigned wc = w >= HO_COPY ? w - HO_COPY : w;
                    if (wc < 16384 && (wc & 8191u) < 7168) {
                        const unsigned uu = wc & 8191u;
                        v = *(const u32x4*)(p.mem0 + (2 * ((uu >> 6) & 3) + ((uu >> 4) & 1)) * DM + 16 * (uu >> 8) + 8 * ((uu >> 5) & 1));
                    }
                }
                __builtin_amdgcn_raw_buffer_store_b128(v, rs, ho_step_base(s) + w, 0, 16);
                if (g < 4) __builtin_amdgcn_raw_buffer_store_b128(v, rs, HO_WC + w, 0, 16);
            }
#pragma unroll
            for (int i = 0; i < 3; ++i) {
                const unsigned g = 4 + (unsigned)m * 3 + i;
                if (g < 260) __builtin_amdgcn_raw_buffer_store_b128(u32x4{SENT, SENT, SENT, SENT}, rs, HO_WC + g * 4096 + tid * 16, 0, 16);
            }
            if (m < 76) {
                const size_t o = m < 56 ? WS_MG + (size_t)31 * 229376 + m * 4096 : m < 72 ? WS_PX + 62 * 32768 + (m - 56) * 4096 : WS_PM + 62 * 8192 + (m - 72) * 4096;
                *(u32x4*)(p.ws + o + tid * 16) = u32x4{SENT, SENT, SENT, SENT};
            }
#pragma unroll
            for (int z = 0; z < 8; ++z) {
                const int g = m * 32 + wave * 8 + z;
                st_wt(p.ws, (g < 1792 ? WS_XF : WS_BXF) + (size_t)(g % 1792) * 1024 + lane * 16, xv[z]);
            }
            if (m < 48) {
                const float sc = which < 2 ? LOG2E : 1.0f;
#pragma unroll
                for (int z = 0; z < 4; ++z) {
                    const int kk = wave + 4 * z < KK ? wave + 4 * z : wave;
#pragma unroll
                    for (int t = 0; t < 2; ++t) {
                        f32x4 d0 = mfma32(wa[z][0], ph[t], f32x4{0, 0, 0, 0}), d1 = mfma32(wa[z][1], ph[t], f32x4{0, 0, 0, 0});
                        st_wt(p.ws, WS_BW + ((size_t)((which * 16 + hb) * 2 + t) * KK + kk) * 1024 + lane * 16, pk8(d0 * sc, d1 * sc));
                    }
                }
            }
        }
    }
}
__device__ __forceinline__ unsigned load_parity(const unsigned char* ws) {
    const unsigned* c = (const unsigned*)(ws + WS_CTRL);
    int v[8], sum = 0;
#pragma unroll
    for (int i = 0; i < 8; ++i) { v[i] = (int)c[i]; int w = v[i] > 600 ? 600 : v[i] < -600 ? -600 : v[i]; sum += (i & 1) ? -w : w; }
    unsigned r = 0;
#pragma unroll
    for (int i = 0; i < 8; ++i) { unsigned bit = ((sum > 0 ? 1u : 0u) ^ i) & 1u; r |= bit << i; }
    return __builtin_amdgcn_readfirstlane(r);
}

__device__ __forceinline__ float xq_max(float x) {
    auto a = __builtin_amdgcn_permlane16_swap(__float_as_uint(x), __float_as_uint(x), false, false);
    x = fmaxf(__uint_as_float(a[0]), __uint_as_float(a[1]));
    auto b = __builtin_amdgcn_permlane32_swap(__float_as_uint(x), __float_as_uint(x), false, false);
    return fmaxf(__uint_as_float(b[0]), __uint_as_float(b[1]));
}
__device__ __forceinline__ float xq_sum(float x) {
    auto a = __builtin_amdgcn_permlane16_swap(__float_as_uint(x), __float_as_uint(x), false, false);
    x = __uint_as_float(a[0]) + __uint_as_float(a[1]);
    auto b = __builtin_amdgcn_permlane32_swap(__float_as_uint(x), __float_as_uint(x), false, false);
    return __uint_as_float(b[0]) + __uint_as_float(b[1]);
}

#define TAU1_T 10
#define TAU2_T 10
#define DEC_T 7
#define DLY_T 0
#define TOLR_T 24
#define OUT_T 3
#define FS_T 4
#define PH_T 3
#define STEP_T 3
#define SCORE_T 8
#define QUIET_T 10
#define HOLD0_T 5
#define HOLD_T 3
#define QS_LOG2 6
#define TOL_T 8
constexpr unsigned SPIN_CAP = 1u << 16;

#define CHAIN_MAIN_ASM \
    "s_mov_b32 s52, 0\n\t" \
    "buffer_load_dwordx4 v[0:3], %[oax], %[rsws], s52 offen offset:0\n\t" \
    "buffer_load_dwordx4 v[32:35], %[oay], %[rsws], s52 offen offset:0\n\t" \
    "buffer_load_dwordx4 v[4:7], %[oax], %[rsws], s52 offen offset:1024\n\t" \
    "buffer_load_dwordx4 v[36:39], %[oay], %[rsws], s52 offen offset:1024\n\t" \
    "s_mov_b32 s52, 14336\n\t" \
    "buffer_load_dwordx4 v[8:11], %[oax], %[rsws], s52 offen offset:0\n\t" \
    "buffer_load_dwordx4 v[40:43], %[oay], %[rsws], s52 offen offset:0\n\t" \
    "buffer_load_dwordx4 v[12:15], %[oax], %[rsws], s52 offen offset:1024\n\t" \
    "buffer_load_dwordx4 v[44:47], %[oay], %[rsws], s52 offen offset:1024\n\t" \
    "s_mov_b32 s52, 28672\n\t" \
    "buffer_load_dwordx4 v[16:19], %[oax], %[rsws], s52 offen offset:0\n\t" \
    "buffer_load_dwordx4 v[48:51], %[oay], %[rsws], s52 offen offset:0\n\t" \
    "buffer_load_dwordx4 v[20:23], %[oax], %[rsws], s52 offen offset:1024\n\t" \
    "buffer_load_dwordx4 v[52:55], %[oay], %[rsws], s52 offen offset:1024\n\t" \
    "s_mov_b32 s52, 43008\n\t" \
    "buffer_load_dwordx4 v[24:27], %[oax], %[rsws], s52 offen offset:0\n\t" \
    "buffer_load_dwordx4 v[56:59], %[oay], %[rsws], s52 offen offset:0\n\t" \
    "buffer_load_dwordx4 v[28:31], %[oax], %[rsws], s52 offen offset:1024\n\t" \
    "buffer_load_dwordx4 v[60:63], %[oay], %[rsws], s52 offen offset:1024\n\t" \
    "s_mov_b32 s52, 0\n\t" \
    "buffer_load_dwordx4 a[0:3], %[ob], %[rsws], s52 offen offset:0\n\t" \
    "buffer_load_dwordx4 a[4:7], %[ob], %[rsws], s52 offen offset:1024\n\t" \
    "buffer_load_dwordx4 a[8:11], %[ob], %[rsws], s52 offen offset:2048\n\t" \
    "buffer_load_dwordx4 a[12:15], %[ob], %[rsws], s52 offen offset:3072\n\t" \
    "s_mov_b32 s52, 4096\n\t" \
    "buffer_load_dwordx4 a[16:19], %[ob], %[rsws], s52 offen offset:0\n\t" \
    "buffer_load_dwordx4 a[20:23], %[ob], %[rsws], s52 offen offset:1024\n\t" \
    "buffer_load_dwordx4 a[24:27], %[ob], %[rsws], s52 offen offset:2048\n\t" \
    "buffer_load_dwordx4 a[28:31], %[ob], %[rsws], s52 offen offset:3072\n\t" \
    "s_mov_b32 s52, 8192\n\t" \
    "buffer_load_dwordx4 a[32:35], %[ob], %[rsws], s52 offen offset:0\n\t" \
    "buffer_load_dwordx4 a[36:39], %[ob], %[rsws], s52 offen offset:1024\n\t" \
    "buffer_load_dwordx4 a[40:43], %[ob], %[rsws], s52 offen offset:2048\n\t" \
    "buffer_load_dwordx4 a[44:47], %[ob], %[rsws], s52 offen offset:3072\n\t" \
    "s_mov_b32 s52, 12288\n\t" \
    "buffer_load_dwordx4 a[48:51], %[ob], %[rsws], s52 offen offset:0\n\t" \
    "buffer_load_dwordx4 a[52:55], %[ob], %[rsws], s52 offen offset:1024\n\t" \
    "s_waitcnt vmcnt(20)\n\t" \
    "s_mov_b32 s52, 401408\n\t" \
    "buffer_load_dwordx4 a[56:59], %[ob], %[rsws], s52 offen offset:0\n\t" \
    "buffer_load_dwordx4 a[60:63], %[ob], %[rsws], s52 offen offset:1024\n\t" \
    "buffer_load_dwordx4 a[64:67], %[ob], %[rsws], s52 offen offset:2048\n\t" \
    "buffer_load_dwordx4 a[68:71], %[ob], %[rsws], s52 offen offset:3072\n\t" \
    "s_mov_b32 s52, 405504\n\t" \
    "buffer_load_dwordx4 a[72:75], %[ob], %[rsws], s52 offen offset:0\n\t" \
    "buffer_load_dwordx4 a[76:79], %[ob], %[rsws], s52 offen offset:1024\n\t" \
    "buffer_load_dwordx4 a[80:83], %[ob], %[rsws], s52 offen offset:2048\n\t" \
    "buffer_load_dwordx4 a[84:87], %[ob], %[rsws], s52 offen offset:3072\n\t" \
    "s_mov_b32 s52, 409600\n\t" \
    "buffer_load_dwordx4 a[88:91], %[ob], %[rsws], s52 offen offset:0\n\t" \
    "buffer_load_dwordx4 a[92:95], %[ob], %[rsws], s52 offen offset:1024\n\t" \
    "buffer_load_dwordx4 a[96:99], %[ob], %[rsws], s52 offen offset:2048\n\t" \
    "buffer_load_dwordx4 a[100:103], %[ob], %[rsws], s52 offen offset:3072\n\t" \
    "s_mov_b32 s52, 413696\n\t" \
    "buffer_load_dwordx4 a[104:107], %[ob], %[rsws], s52 offen offset:0\n\t" \
    "buffer_load_dwordx4 a[108:111], %[ob], %[rsws], s52 offen offset:1024\n\t" \
    "s_waitcnt vmcnt(20)\n\t" \
    "s_mov_b32 s52, 802816\n\t" \
    "buffer_load_dwordx4 a[112:115], %[ob], %[rsws], s52 offen offset:0\n\t" \
    "buffer_load_dwordx4 a[116:119], %[ob], %[rsws], s52 offen offset:1024\n\t" \
    "buffer_load_dwordx4 a[120:123], %[ob], %[rsws], s52 offen offset:2048\n\t" \
    "buffer_load_dwordx4 a[124:127], %[ob], %[rsws], s52 offen offset:3072\n\t" \
    "s_mov_b32 s52, 806912\n\t" \
    "buffer_load_dwordx4 a[128:131], %[ob], %[rsws], s52 offen offset:0\n\t" \
    "buffer_load_dwordx4 a[132:135], %[ob], %[rsws], s52 offen offset:1024\n\t" \
    "buffer_load_dwordx4 a[136:139], %[ob], %[rsws], s52 offen offset:2048\n\t" \
    "buffer_load_dwordx4 a[140:143], %[ob], %[rsws], s52 offen offset:3072\n\t" \
    "s_mov_b32 s52, 811008\n\t" \
    "buffer_load_dwordx4 a[144:147], %[ob], %[rsws], s52 offen offset:0\n\t" \
    "buffer_load_dwordx4 a[148:151], %[ob], %[rsws], s52 offen offset:1024\n\t" \
    "buffer_load_dwordx4 a[152:155], %[ob], %[rsws], s52 offen offset:2048\n\t" \
    "buffer_load_dwordx4 a[156:159], %[ob], %[rsws], s52 offen offset:3072\n\t" \
    "s_mov_b32 s52, 815104\n\t" \
    "buffer_load_dwordx4 a[160:163], %[ob], %[rsws], s52 offen offset:0\n\t" \
    "buffer_load_dwordx4 a[164:167], %[ob], %[rsws], s52 offen offset:1024\n\t" \
    "s_waitcnt vmcnt(20)\n\t" \
    "s_mov_b32 s52, 1204224\n\t" \
    "buffer_load_dwordx4 a[168:171], %[ob], %[rsws], s52 offen offset:0\n\t" \
    "buffer_load_dwordx4 a[172:175], %[ob], %[rsws], s52 offen offset:1024\n\t" \
    "buffer_load_dwordx4 a[176:179], %[ob], %[rsws], s52 offen offset:2048\n\t" \
    "buffer_load_dwordx4 a[180:183], %[ob], %[rsws], s52 offen offset:3072\n\t" \
    "s_mov_b32 s52, 1208320\n\t" \
    "buffer_load_dwordx4 a[184:187], %[ob], %[rsws], s52 offen offset:0\n\t" \
    "buffer_load_dwordx4 a[188:191], %[ob], %[rsws], s52 offen offset:1024\n\t" \
    "buffer_load_dwordx4 a[192:195], %[ob], %[rsws], s52 offen offset:2048\n\t" \
    "buffer_load_dwordx4 a[196:199], %[ob], %[rsws], s52 offen offset:3072\n\t" \
    "s_mov_b32 s52, 1212416\n\t" \
    "buffer_load_dwordx4 a[200:203], %[ob], %[rsws], s52 offen offset:0\n\t" \
    "buffer_load_dwordx4 a[204:207], %[ob], %[rsws], s52 offen offset:1024\n\t" \
    "buffer_load_dwordx4 a[208:211], %[ob], %[rsws], s52 offen offset:2048\n\t" \
    "buffer_load_dwordx4 a[212:215], %[ob], %[rsws], s52 offen offset:3072\n\t" \
    "s_mov_b32 s52, 1216512\n\t" \
    "buffer_load_dwordx4 a[216:219], %[ob], %[rsws], s52 offen offset:0\n\t" \
    "buffer_load_dwordx4 a[220:223], %[ob], %[rsws], s52 offen offset:1024\n\t" \
    "s_waitcnt vmcnt(20)\n\t" \
    "s_and_b32 s50, %[par], 1\n\t" \
    "s_xor_b32 s50, s50, FAR_\n\t" \
    "s_lshl_b32 s50, s50, 13\n\t" \
    "s_mov_b32 s90, 0\n\t" \
    "s_waitcnt vmcnt(0)\n\t" \
    "v_and_b32 v32, %[ymask], v32\n\t" \
    "v_and_b32 v33, %[ymask], v33\n\t" \
    "v_and_b32 v34, %[ymask], v34\n\t" \
    "v_and_b32 v35, %[ymask], v35\n\t" \
    "v_and_b32 v36, %[ymask], v36\n\t" \
    "v_and_b32 v37, %[ymask], v37\n\t" \
    "v_and_b32 v38, %[ymask], v38\n\t" \
    "v_and_b32 v39, %[ymask], v39\n\t" \
    "v_and_b32 v40, %[ymask], v40\n\t" \
    "v_and_b32 v41, %[ymask], v41\n\t" \
    "v_and_b32 v42, %[ymask], v42\n\t" \
    "v_and_b32 v43, %[ymask], v43\n\t" \
    "v_and_b32 v44, %[ymask], v44\n\t" \
    "v_and_b32 v45, %[ymask], v45\n\t" \
    "v_and_b32 v46, %[ymask], v46\n\t" \
    "v_and_b32 v47, %[ymask], v47\n\t" \
    "v_and_b32 v48, %[ymask], v48\n\t" \
    "v_and_b32 v49, %[ymask], v49\n\t" \
    "v_and_b32 v50, %[ymask], v50\n\t" \
    "v_and_b32 v51, %[ymask], v51\n\t" \
    "v_and_b32 v52, %[ymask], v52\n\t" \
    "v_and_b32 v53, %[ymask], v53\n\t" \
    "v_and_b32 v54, %[ymask], v54\n\t" \
    "v_and_b32 v55, %[ymask], v55\n\t" \
    "v_and_b32 v56, %[ymask], v56\n\t" \
    "v_and_b32 v57, %[ymask], v57\n\t" \
    "v_and_b32 v58, %[ymask], v58\n\t" \
    "v_and_b32 v59, %[ymask], v59\n\t" \
    "v_and_b32 v60, %[ymask], v60\n\t" \
    "v_and_b32 v61, %[ymask], v61\n\t" \
    "v_and_b32 v62, %[ymask], v62\n\t" \
    "v_and_b32 v63, %[ymask], v63\n\t" \
    "s_mov_b32 s45, 0\n\t" \
    "s_mov_b32 s46, 0\n\t" \
    "s_mov_b32 s47, 0\n\t" \
    "s_mov_b32 s49, 0\n\t" \
    "s_and_b32 s42, %[par], 1\n\t" \
    "s_lshl_b32 s42, s42, 13\n\t" \
    "s_mov_b32 s46, s42\n\t" \
    "s_add_u32 s47, s42, 0x200000\n\t" \
    "s_mov_b32 s88, 0x40000000\n\t" \
    "s_mov_b32 s99, 0x40000000\n\t" \
    "v_mov_b32 v199, SENT_\n\t" \
    "v_accvgpr_write_b32 a240, v199\n\t" \
    "v_accvgpr_write_b32 a241, v199\n\t" \
    "v_accvgpr_write_b32 a242, v199\n\t" \
    "v_accvgpr_write_b32 a243, v199\n\t" \
    "s_mov_b32 s59, 0x40000000\n\t" \
    "s_mov_b32 s60, 0x40000000\n\t" \
    "s_mov_b32 s89, 0x40000000\n\t" \
    "s_mov_b32 s61, C1_\n\t" \
    "s_mov_b32 s62, C2_\n\t" \
    "s_nop 0\n\t" \
    "s_mov_b32 s87, 0\n\t" \
    "s_mov_b32 s49, 0\n\t" \
    "LCH_c0%=:\n\t" \
    "buffer_load_dword v198, %[ocnt], %[rsho], s87 offen sc1\n\t" \
    "s_waitcnt vmcnt(0)\n\t" \
    "v_readfirstlane_b32 s52, v198\n\t" \
    "s_add_u32 s49, s49, 1\n\t" \
    "s_cmp_gt_u32 s49, SPIN2_\n\t" \
    "s_cbranch_scc1 LCH_end%=\n\t" \
    "s_cmp_lt_i32 s52, NPROD_\n\t" \
    "s_cbranch_scc1 LCH_c0%=\n\t" \
    "s_mov_b32 s49, 0\n\t" \
    "s_memrealtime s[54:55]\n\t" \
    "s_waitcnt lgkmcnt(0)\n\t" \
    "s_lshr_b32 s41, s54, QS_LOG\n\t" \
    "s_add_u32 s41, s41, 1\n\t" \
    "s_lshl_b32 s40, s41, QS_LOG\n\t" \
    "s_mov_b32 s84, 0\n\t" \
    "s_mov_b32 s85, 0\n\t" \
    "s_mov_b32 s63, C1_-TAU1_\n\t" \
    "s_mov_b32 s81, C2_-TAU2_\n\t" \
    "s_mov_b32 s91, 0x40000000\n\t" \
    "s_mov_b32 s92, C1_-TAU1_\n\t" \
    "s_mov_b32 s93, C2_-TAU2_\n\t" \
    "s_mov_b32 s94, 0\n\t" \
    "s_mov_b32 s95, 0\n\t" \
    "s_mov_b32 s96, 0\n\t" \
    "s_mov_b32 s97, 0\n\t" \
    "s_mov_b32 s86, 64\n\t" \
    "s_mov_b32 s87, 64\n\t" \
    "v_add_u32 v171, 1280, %[pw]\n\t" \
    "v_mov_b32 v200, 0\n\t" \
    "v_mov_b32 v201, 0\n\t" \
    ".p2align 6\n\t" \
    "LCH_loop%=:\n\t" \
    "LCH_A%=_w0:\n\t" \
    "s_bitcmp1_b32 s41, 0\n\t" \
    "s_cselect_b32 s44, HO_COPY_, 0\n\t" \
    "s_add_u32 s44, s44, s42\n\t" \
    "LCH_A%=_w1:\n\t" \
    "s_memrealtime s[54:55]\n\t" \
    "s_waitcnt lgkmcnt(0)\n\t" \
    "s_sub_u32 s48, s54, s40\n\t" \
    "s_cmp_lt_i32 s48, -PH_\n\t" \
    "s_cbranch_scc1 LCH_A%=_w1\n\t" \
    "s_cmp_gt_i32 s48, TOL_\n\t" \
    "s_cbranch_scc1 LCH_A%=_slip\n\t" \
    "LCH_A%=_w2:\n\t" \
    "s_add_i32 s48, s48, 1\n\t" \
    "s_cmp_le_i32 s48, 0\n\t" \
    "s_cbranch_scc1 LCH_A%=_w2\n\t" \
    "buffer_load_dwordx4 v[64:67], %[ora], %[rsho], s44 offen sc1\n\t" \
    "buffer_load_dwordx4 v[68:71], %[orb], %[rsho], s44 offen sc1\n\t" \
    "v_mov_b32 v140, 0\n\t" \
    "v_mov_b32 v141, 0\n\t" \
    "v_mov_b32 v142, 0\n\t" \
    "v_mov_b32 v143, 0\n\t" \
    "v_mov_b32 v144, 0\n\t" \
    "v_mov_b32 v145, 0\n\t" \
    "v_mov_b32 v146, 0\n\t" \
    "v_mov_b32 v147, 0\n\t" \
    "v_mov_b32 v148, 0\n\t" \
    "v_mov_b32 v149, 0\n\t" \
    "v_mov_b32 v150, 0\n\t" \
    "v_mov_b32 v151, 0\n\t" \
    "v_mov_b32 v152, 0\n\t" \
    "v_mov_b32 v153, 0\n\t" \
    "v_mov_b32 v154, 0\n\t" \
    "v_mov_b32 v155, 0\n\t" \
    "buffer_load_dwordx4 a[224:227], %[ou], %[rsho], s50 offen offset:0 sc1\n\t" \
    "buffer_load_dwordx4 a[228:231], %[ou], %[rsho], s50 offen offset:1024 sc1\n\t" \
    "buffer_load_dwordx4 a[232:235], %[ou], %[rsho], s50 offen offset:2048 sc1\n\t" \
    "buffer_load_dwordx4 a[236:239], %[ou], %[rsho], s50 offen offset:3072 sc1\n\t" \
    "buffer_load_dword v204, %[opar], %[rsho], s86 offen sc1\n\t" \
    "buffer_load_dword v205, %[ocnt], %[rsho], s87 offen sc1\n\t" \
    "s_lshl_b32 s53, s42, 1\n\t" \
    "s_add_u32 s53, s53, HO_COPY_\n\t" \
    "s_sub_u32 s98, s53, s44\n\t" \
    "s_cmp_gt_u32 %[wv], 1\n\t" \
    "s_cbranch_scc1 LCH_wst%=\n\t" \
    "s_waitcnt vmcnt(7)\n\t" \
    "v_and_b32 v72, %[mlo], v64\n\t" \
    "v_and_b32 v73, %[mlo], v65\n\t" \
    "v_and_b32 v74, %[mlo], v66\n\t" \
    "v_and_b32 v75, %[mlo], v67\n\t" \
    "v_and_b32 v76, %[mhi], v64\n\t" \
    "v_and_b32 v77, %[mhi], v65\n\t" \
    "v_mfma_f32_16x16x32_bf16 v[88:91], v[32:35], v[72:75], 0\n\t" \
    "v_and_b32 v78, %[mhi], v66\n\t" \
    "v_and_b32 v79, %[mhi], v67\n\t" \
    "v_mfma_f32_16x16x32_bf16 v[92:95], v[40:43], v[72:75], 0\n\t" \
    "s_nop 0\n\t" \
    "v_mfma_f32_16x16x32_bf16 v[88:91], v[36:39], v[76:79], v[88:91]\n\t" \
    "s_waitcnt vmcnt(6)\n\t" \
    "LCH_Ago2%=:\n\t" \
    "v_and_b32 v80, %[mlo], v68\n\t" \
    "v_and_b32 v81, %[mlo], v69\n\t" \
    "v_mfma_f32_16x16x32_bf16 v[92:95], v[44:47], v[76:79], v[92:95]\n\t" \
    "v_and_b32 v82, %[mlo], v70\n\t" \
    "v_and_b32 v83, %[mlo], v71\n\t" \
    "v_mfma_f32_16x16x32_bf16 v[96:99], v[48:51], v[72:75], 0\n\t" \
    "v_and_b32 v84, %[mhi], v68\n\t" \
    "v_and_b32 v85, %[mhi], v69\n\t" \
    "v_mfma_f32_16x16x32_bf16 v[100:103], v[56:59], v[72:75], 0\n\t" \
    "v_and_b32 v86, %[mhi], v70\n\t" \
    "v_and_b32 v87, %[mhi], v71\n\t" \
    "v_mfma_f32_16x16x32_bf16 v[96:99], v[52:55], v[76:79], v[96:99]\n\t" \
    "v_mfma_f32_16x16x32_bf16 v[100:103], v[60:63], v[76:79], v[100:103]\n\t" \
    "v_cvt_pk_bf16_f32 v120, v88, v89\n\t" \
    "v_cvt_pk_bf16_f32 v121, v90, v91\n\t" \
    "v_mfma_f32_16x16x32_bf16 v[104:107], v[0:3], v[80:83], 0\n\t" \
    "v_cvt_pk_bf16_f32 v122, v92, v93\n\t" \
    "v_cvt_pk_bf16_f32 v123, v94, v95\n\t" \
    "v_mfma_f32_16x16x32_bf16 v[108:111], v[8:11], v[80:83], 0\n\t" \
    "v_cvt_pk_bf16_f32 v124, v96, v97\n\t" \
    "v_cvt_pk_bf16_f32 v125, v98, v99\n\t" \
    "v_mfma_f32_16x16x32_bf16 v[104:107], v[4:7], v[84:87], v[104:107]\n\t" \
    "v_cvt_pk_bf16_f32 v126, v100, v101\n\t" \
    "v_cvt_pk_bf16_f32 v127, v102, v103\n\t" \
    "v_mfma_f32_16x16x32_bf16 v[108:111], v[12:15], v[84:87], v[108:111]\n\t" \
    "ds_write2_b32 %[mwa], v64, v66 offset0:0 offset1:1\n\t" \
    "v_mfma_f32_16x16x32_bf16 v[112:115], v[16:19], v[80:83], 0\n\t" \
    "ds_write2_b32 %[mwa], v65, v67 offset0:32 offset1:33\n\t" \
    "v_mfma_f32_16x16x32_bf16 v[116:119], v[24:27], v[80:83], 0\n\t" \
    "ds_write2_b32 %[mwb], v68, v70 offset0:0 offset1:1\n\t" \
    "v_mfma_f32_16x16x32_bf16 v[112:115], v[20:23], v[84:87], v[112:115]\n\t" \
    "v_cvt_pk_bf16_f32 v128, v104, v105\n\t" \
    "v_cvt_pk_bf16_f32 v129, v106, v107\n\t" \
    "v_mfma_f32_16x16x32_bf16 v[116:119], v[28:31], v[84:87], v[116:119]\n\t" \
    "v_cvt_pk_bf16_f32 v130, v108, v109\n\t" \
    "v_cvt_pk_bf16_f32 v131, v110, v111\n\t" \
    "v_mfma_f32_16x16x32_bf16 v[136:139], v[80:83], v[120:123], 0\n\t" \
    "ds_write2_b32 %[mwb], v69, v71 offset0:32 offset1:33\n\t" \
    "v_mfma_f32_16x16x32_bf16 v[136:139], v[84:87], v[124:127], v[136:139]\n\t" \
    "v_cvt_pk_bf16_f32 v132, v112, v113\n\t" \
    "v_cvt_pk_bf16_f32 v133, v114, v115\n\t" \
    "v_mfma_f32_16x16x32_bf16 v[136:139], v[72:75], v[128:131], v[136:139]\n\t" \
    "v_cvt_pk_bf16_f32 v134, v116, v117\n\t" \
    "v_cvt_pk_bf16_f32 v135, v118, v119\n\t" \
    "s_nop 1\n\t" \
    "v_mfma_f32_16x16x32_bf16 v[136:139], v[76:79], v[132:135], v[136:139]\n\t" \
    "s_nop 7\n\t" \
    "ds_write2_b32 %[pw], v136, v137 offset0:0 offset1:160\n\t" \
    "ds_write2_b32 v171, v138, v139 offset0:0 offset1:160\n\t" \
    "v_cmp_u_f32 vcc, v136, v137\n\t" \
    "v_cmp_u_f32 s[56:57], v138, v139\n\t" \
    "s_or_b64 vcc, vcc, s[56:57]\n\t" \
    "s_cbranch_vccnz LCH_Afail%=\n\t" \
    "LCH_Aok%=:\n\t" \
    "s_cmp_lg_u32 %[wv], 0\n\t" \
    "s_waitcnt lgkmcnt(0)\n\t" \
    "s_barrier\n\t" \
    "s_cbranch_scc1 LCH_sk1%=\n\t" \
    "ds_read_b128 v[176:179], %[prd] offset:0\n\t" \
    "ds_read_b128 v[180:183], %[prd] offset:16\n\t" \
    "ds_read_b128 v[184:187], %[prd] offset:32\n\t" \
    "ds_read_b128 v[188:191], %[prd] offset:48\n\t" \
    "ds_read_b32 v196, %[nadr]\n\t" \
    "s_waitcnt lgkmcnt(3)\n\t" \
    "v_pk_add_f32 v[176:177], v[176:177], v[180:181]\n\t" \
    "v_pk_add_f32 v[178:179], v[178:179], v[182:183]\n\t" \
    "s_waitcnt lgkmcnt(1)\n\t" \
    "v_pk_add_f32 v[184:185], v[184:185], v[188:189]\n\t" \
    "v_pk_add_f32 v[186:187], v[186:187], v[190:191]\n\t" \
    "v_pk_add_f32 v[176:177], v[176:177], v[178:179]\n\t" \
    "v_pk_add_f32 v[184:185], v[184:185], v[186:187]\n\t" \
    "v_pk_add_f32 v[176:177], v[176:177], v[184:185]\n\t" \
    "v_add_f32 v192, v176, v177\n\t" \
    "s_mov_b32 s56, 0x00FF01FF\n\t" \
    "s_mov_b32 s57, 0x00FF00FF\n\t" \
    "v_mov_b32_dpp v193, v192 row_shl:8 row_mask:0xf bank_mask:0xf\n\t" \
    "v_writelane_b32 v192, s90, 8\n\t" \
    "s_mov_b64 exec, s[56:57]\n\t" \
    "buffer_store_dwordx2 v[192:193], %[osa], %[rsho], s44 offen sc1\n\t" \
    "buffer_store_dwordx2 v[192:193], %[osa], %[rsho], s98 offen sc1\n\t" \
    "s_mov_b64 exec, -1\n\t" \
    "s_memrealtime s[82:83]\n\t" \
    "LCH_bk1%=:\n\t" \
    "s_waitcnt lgkmcnt(0)\n\t" \
    "v_readfirstlane_b32 s52, v196\n\t" \
    "s_nop 0\n\t" \
    "s_sub_i32 s52, s52, s41\n\t" \
    "s_max_i32 s52, s52, 0\n\t" \
    "s_add_u32 s41, s41, s52\n\t" \
    "s_lshl_b32 s52, s52, QS_LOG\n\t" \
    "s_add_u32 s40, s40, s52\n\t" \
    "s_sub_u32 s84, s82, s40\n\t" \
    "s_add_u32 s40, s40, s61\n\t" \
    "s_mov_b32 s49, 0\n\t" \
    "s_cmp_eq_u32 s45, NSTEP_\n\t" \
    "s_cbranch_scc1 LCH_epi%=\n\t" \
    "ds_read_b128 v[88:91], %[mrd] offset:0\n\t" \
    "ds_read_b128 v[92:95], %[mrd] offset:1024\n\t" \
    "ds_read_b128 v[96:99], %[mrd] offset:2048\n\t" \
    "ds_read_b128 v[100:103], %[mrd] offset:3072\n\t" \
    "ds_read_b128 v[104:107], %[mrd] offset:4096\n\t" \
    "ds_read_b128 v[108:111], %[mrd] offset:5120\n\t" \
    "ds_read_b128 v[112:115], %[mrd] offset:6144\n\t" \
    "LCH_C%=_w0:\n\t" \
    "s_bitcmp1_b32 s41, 0\n\t" \
    "s_cselect_b32 s44, HO_COPY_, 0\n\t" \
    "s_add_u32 s44, s44, s42\n\t" \
    "LCH_C%=_w1:\n\t" \
    "s_memrealtime s[54:55]\n\t" \
    "s_waitcnt lgkmcnt(0)\n\t" \
    "s_sub_u32 s48, s54, s40\n\t" \
    "s_cmp_lt_i32 s48, -PH_\n\t" \
    "s_cbranch_scc1 LCH_C%=_w1\n\t" \
    "s_cmp_gt_i32 s48, TOL_\n\t" \
    "s_cbranch_scc1 LCH_C%=_slip\n\t" \
    "LCH_C%=_w2:\n\t" \
    "s_add_i32 s48, s48, 1\n\t" \
    "s_cmp_le_i32 s48, 0\n\t" \
    "s_cbranch_scc1 LCH_C%=_w2\n\t" \
    "buffer_load_dword v164, %[osmm], %[rsho], s44 offen offset:0 sc1\n\t" \
    "buffer_load_dword v165, %[osmm], %[rsho], s44 offen offset:64 sc1\n\t" \
    "buffer_load_dword v166, %[osmm], %[rsho], s44 offen offset:128 sc1\n\t" \
    "buffer_load_dword v167, %[osmm], %[rsho], s44 offen offset:192 sc1\n\t" \
    "buffer_load_dword v168, %[osmm], %[rsho], s44 offen offset:256 sc1\n\t" \
    "buffer_load_dword v169, %[osmm], %[rsho], s44 offen offset:320 sc1\n\t" \
    "buffer_load_dword v170, %[osmm], %[rsho], s44 offen offset:384 sc1\n\t" \
    "buffer_load_dwordx4 v[172:175], %[osx], %[rsho], s44 offen sc1\n\t" \
    "v_smfmac_f32_16x16x64_bf16 v[140:143], v[88:91], a[0:7], %[sidx]\n\t" \
    "s_add_u32 s53, s45, 1\n\t" \
    "s_and_b32 s52, s53, 3\n\t" \
    "v_smfmac_f32_16x16x64_bf16 v[144:147], v[88:91], a[56:63], %[sidx]\n\t" \
    "s_mul_i32 s43, s52, HO_STEP_\n\t" \
    "s_add_u32 s43, s43, s46\n\t" \
    "v_smfmac_f32_16x16x64_bf16 v[148:151], v[88:91], a[112:119], %[sidx]\n\t" \
    "s_lshl_b32 s52, s53, 14\n\t" \
    "s_add_u32 s52, s52, s47\n\t" \
    "v_smfmac_f32_16x16x64_bf16 v[152:155], v[88:91], a[168:175], %[sidx]\n\t" \
    "s_mov_b32 s99, s52\n\t" \
    "s_mul_i32 s89, s45, 229376\n\t" \
    "v_smfmac_f32_16x16x64_bf16 v[140:143], v[92:95], a[8:15], %[sidx]\n\t" \
    "s_add_u32 s53, s45, 1\n\t" \
    "s_lshr_b32 s52, s53, 1\n\t" \
    "v_smfmac_f32_16x16x64_bf16 v[144:147], v[92:95], a[64:71], %[sidx]\n\t" \
    "s_xor_b32 s52, s52, %[par]\n\t" \
    "s_and_b32 s52, s52, 1\n\t" \
    "v_smfmac_f32_16x16x64_bf16 v[148:151], v[92:95], a[120:127], %[sidx]\n\t" \
    "s_xor_b32 s52, s52, FAR_\n\t" \
    "s_lshl_b32 s52, s52, 13\n\t" \
    "v_smfmac_f32_16x16x64_bf16 v[152:155], v[92:95], a[176:183], %[sidx]\n\t" \
    "s_lshl_b32 s50, s53, 20\n\t" \
    "s_add_u32 s50, s50, s52\n\t" \
    "v_smfmac_f32_16x16x64_bf16 v[140:143], v[96:99], a[16:23], %[sidx]\n\t" \
    "s_add_u32 s59, s45, 2\n\t" \
    "s_lshl_b32 s60, s59, 6\n\t" \
    "v_smfmac_f32_16x16x64_bf16 v[144:147], v[96:99], a[72:79], %[sidx]\n\t" \
    "s_lshl_b32 s59, s59, 6\n\t" \
    "s_min_u32 s90, s84, 255\n\t" \
    "v_smfmac_f32_16x16x64_bf16 v[148:151], v[96:99], a[128:135], %[sidx]\n\t" \
    "s_min_u32 s52, s85, 255\n\t" \
    "s_lshl_b32 s52, s52, 8\n\t" \
    "v_smfmac_f32_16x16x64_bf16 v[152:155], v[96:99], a[184:191], %[sidx]\n\t" \
    "s_or_b32 s90, s90, s52\n\t" \
    "v_smfmac_f32_16x16x64_bf16 v[140:143], v[100:103], a[24:31], %[sidx]\n\t" \
    "v_smfmac_f32_16x16x64_bf16 v[144:147], v[100:103], a[80:87], %[sidx]\n\t" \
    "v_smfmac_f32_16x16x64_bf16 v[148:151], v[100:103], a[136:143], %[sidx]\n\t" \
    "v_smfmac_f32_16x16x64_bf16 v[152:155], v[100:103], a[192:199], %[sidx]\n\t" \
    "v_smfmac_f32_16x16x64_bf16 v[140:143], v[104:107], a[32:39], %[sidx]\n\t" \
    "v_smfmac_f32_16x16x64_bf16 v[144:147], v[104:107], a[88:95], %[sidx]\n\t" \
    "v_smfmac_f32_16x16x64_bf16 v[148:151], v[104:107], a[144:151], %[sidx]\n\t" \
    "v_smfmac_f32_16x16x64_bf16 v[152:155], v[104:107], a[200:207], %[sidx]\n\t" \
    "v_smfmac_f32_16x16x64_bf16 v[140:143], v[108:111], a[40:47], %[sidx]\n\t" \
    "v_smfmac_f32_16x16x64_bf16 v[144:147], v[108:111], a[96:103], %[sidx]\n\t" \
    "v_smfmac_f32_16x16x64_bf16 v[148:151], v[108:111], a[152:159], %[sidx]\n\t" \
    "v_smfmac_f32_16x16x64_bf16 v[152:155], v[108:111], a[208:215], %[sidx]\n\t" \
    "v_smfmac_f32_16x16x64_bf16 v[140:143], v[112:115], a[48:55], %[sidx]\n\t" \
    "v_smfmac_f32_16x16x64_bf16 v[144:147], v[112:115], a[104:111], %[sidx]\n\t" \
    "v_smfmac_f32_16x16x64_bf16 v[148:151], v[112:115], a[160:167], %[sidx]\n\t" \
    "v_smfmac_f32_16x16x64_bf16 v[152:155], v[112:115], a[216:223], %[sidx]\n\t" \
    "s_nop 6\n\t" \
    "v_cvt_pk_bf16_f32 v156, v140, v141\n\t" \
    "v_cvt_pk_bf16_f32 v157, v142, v143\n\t" \
    "v_cvt_pk_bf16_f32 v158, v144, v145\n\t" \
    "v_cvt_pk_bf16_f32 v159, v146, v147\n\t" \
    "v_cvt_pk_bf16_f32 v160, v148, v149\n\t" \
    "v_cvt_pk_bf16_f32 v161, v150, v151\n\t" \
    "v_cvt_pk_bf16_f32 v162, v152, v153\n\t" \
    "v_cvt_pk_bf16_f32 v163, v154, v155\n\t" \
    "s_waitcnt vmcnt(0)\n\t" \
    "LCH_Cgo%=:\n\t" \
    "v_pk_add_f32 v[182:183], v[164:165], v[166:167]\n\t" \
    "v_max3_f32 v184, v172, v173, v174\n\t" \
    "v_pk_add_f32 v[182:183], v[182:183], v[168:169]\n\t" \
    "v_add_f32 v180, v182, v170\n\t" \
    "v_add_f32 v180, v180, v183\n\t" \
    "v_max3_f32 v184, v184, v175, v180\n\t" \
    "s_nop 1\n\t" \
    "v_max_f32_dpp v184, v184, v184 quad_perm:[1,0,3,2] row_mask:0xf bank_mask:0xf\n\t" \
    "s_nop 1\n\t" \
    "v_max_f32_dpp v184, v184, v184 quad_perm:[2,3,0,1] row_mask:0xf bank_mask:0xf\n\t" \
    "s_nop 1\n\t" \
    "v_max_f32_dpp v184, v184, v184 row_half_mirror row_mask:0xf bank_mask:0xf\n\t" \
    "v_pk_add_f32 v[176:177], v[172:173], v[184:185] op_sel_hi:[1,0] neg_lo:[0,1] neg_hi:[0,1]\n\t" \
    "v_pk_add_f32 v[178:179], v[174:175], v[184:185] op_sel_hi:[1,0] neg_lo:[0,1] neg_hi:[0,1]\n\t" \
    "v_exp_f32 v176, v176\n\t" \
    "v_sub_f32 v180, v180, v184\n\t" \
    "v_exp_f32 v177, v177\n\t" \
    "v_exp_f32 v178, v178\n\t" \
    "v_exp_f32 v179, v179\n\t" \
    "v_add_f32 v182, v176, v177\n\t" \
    "v_exp_f32 v180, v180\n\t" \
    "v_add_f32 v183, v178, v179\n\t" \
    "v_add_f32 v182, v182, v180\n\t" \
    "v_add_f32 v181, v182, v183\n\t" \
    "s_nop 1\n\t" \
    "v_add_f32_dpp v181, v181, v181 quad_perm:[1,0,3,2] row_mask:0xf bank_mask:0xf\n\t" \
    "s_nop 1\n\t" \
    "v_add_f32_dpp v181, v181, v181 quad_perm:[2,3,0,1] row_mask:0xf bank_mask:0xf\n\t" \
    "s_nop 1\n\t" \
    "v_add_f32_dpp v181, v181, v181 row_half_mirror row_mask:0xf bank_mask:0xf\n\t" \
    "v_rcp_f32 v182, v181\n\t" \
    "v_cmp_u_f32 vcc, v181, v181\n\t" \
    "v_pk_mul_f32 v[176:177], v[176:177], v[182:183] op_sel_hi:[1,0]\n\t" \
    "v_pk_mul_f32 v[178:179], v[178:179], v[182:183] op_sel_hi:[1,0]\n\t" \
    "v_mul_f32 v180, v180, v182\n\t" \
    "v_cvt_pk_bf16_f32 v176, v176, v177\n\t" \
    "v_cvt_pk_bf16_f32 v177, v178, v179\n\t" \
    "v_cvt_pk_bf16_f32 v180, v180, v180\n\t" \
    "ds_write_b64 %[pwx], v[176:177]\n\t" \
    "ds_write_b16 %[pwm], v180\n\t" \
    "ds_read_b128 v[92:95], %[pr] offset:128\n\t" \
    "ds_read_b128 v[96:99], %[pr] offset:256\n\t" \
    "ds_read_b128 v[100:103], %[pr] offset:384\n\t" \
    "ds_read_b128 v[104:107], %[pr] offset:512\n\t" \
    "ds_read_b128 v[88:91], %[prm]\n\t" \
    "ds_read_b128 v[108:111], %[prm] offset:64\n\t" \
    "s_cbranch_vccnz LCH_Cfail%=\n\t" \
    "LCH_Cok%=:\n\t" \
    "s_sub_u32 s53, s44, s42\n\t" \
    "s_add_u32 s51, s43, s53\n\t" \
    "s_sub_u32 s98, HO_COPY_, s53\n\t" \
    "s_add_u32 s98, s98, s43\n\t" \
    "s_waitcnt lgkmcnt(5)\n\t" \
    "v_mfma_f32_16x16x32_bf16 v[136:139], a[224:227], v[92:95], 0\n\t" \
    "s_waitcnt lgkmcnt(4)\n\t" \
    "v_mfma_f32_16x16x32_bf16 v[136:139], a[228:231], v[96:99], v[136:139]\n\t" \
    "s_waitcnt lgkmcnt(3)\n\t" \
    "v_mfma_f32_16x16x32_bf16 v[136:139], a[232:235], v[100:103], v[136:139]\n\t" \
    "s_waitcnt lgkmcnt(2)\n\t" \
    "v_mfma_f32_16x16x32_bf16 v[136:139], a[236:239], v[104:107], v[136:139]\n\t" \
    "s_waitcnt lgkmcnt(1)\n\t" \
    "v_mfma_f32_16x16x32_bf16 v[136:139], v[156:159], v[88:91], v[136:139]\n\t" \
    "s_waitcnt lgkmcnt(0)\n\t" \
    "v_mfma_f32_16x16x32_bf16 v[136:139], v[160:163], v[108:111], v[136:139]\n\t" \
    "s_nop 7\n\t" \
    "ds_write_b128 %[redw], v[136:139]\n\t" \
    "s_cmp_lg_u32 %[wv], 0\n\t" \
    "s_waitcnt lgkmcnt(0)\n\t" \
    "s_barrier\n\t" \
    "s_cbranch_scc1 LCH_sk3%=\n\t" \
    "LCH_fin%=:\n\t" \
    "ds_read_b128 v[176:179], %[redr] offset:0\n\t" \
    "ds_read_b128 v[180:183], %[redr] offset:16\n\t" \
    "ds_read_b128 v[184:187], %[redr] offset:32\n\t" \
    "ds_read_b128 v[188:191], %[redr] offset:48\n\t" \
    "ds_read_b32 v196, %[nadr]\n\t" \
    "s_waitcnt lgkmcnt(3)\n\t" \
    "v_pk_add_f32 v[176:177], v[176:177], v[180:181]\n\t" \
    "v_pk_add_f32 v[178:179], v[178:179], v[182:183]\n\t" \
    "s_waitcnt lgkmcnt(1)\n\t" \
    "v_pk_add_f32 v[184:185], v[184:185], v[188:189]\n\t" \
    "v_pk_add_f32 v[186:187], v[186:187], v[190:191]\n\t" \
    "v_pk_add_f32 v[176:177], v[176:177], v[184:185]\n\t" \
    "v_pk_add_f32 v[178:179], v[178:179], v[186:187]\n\t" \
    "v_cvt_pk_bf16_f32 v192, v176, v177\n\t" \
    "v_cvt_pk_bf16_f32 v193, v178, v179\n\t" \
    "s_mov_b64 exec, 0xff\n\t" \
    "buffer_store_dwordx2 v[192:193], %[oma], %[rsho], s51 offen sc1\n\t" \
    "buffer_store_dwordx2 v[192:193], %[oma], %[rsho], s98 offen sc1\n\t" \
    "s_mov_b64 exec, -1\n\t" \
    "s_memrealtime s[82:83]\n\t" \
    "LCH_bk3%=:\n\t" \
    "s_mov_b32 s88, s42\n\t" \
    "s_mov_b32 s42, s43\n\t" \
    "s_add_u32 s45, s45, 1\n\t" \
    "s_waitcnt lgkmcnt(0)\n\t" \
    "v_readfirstlane_b32 s52, v196\n\t" \
    "s_nop 0\n\t" \
    "s_sub_i32 s52, s52, s41\n\t" \
    "s_max_i32 s52, s52, 0\n\t" \
    "s_add_u32 s41, s41, s52\n\t" \
    "s_lshl_b32 s52, s52, QS_LOG\n\t" \
    "s_add_u32 s40, s40, s52\n\t" \
    "s_sub_u32 s85, s82, s40\n\t" \
    "s_add_u32 s40, s40, s62\n\t" \
    "s_mov_b32 s49, 0\n\t" \
    "v_readfirstlane_b32 s52, v204\n\t" \
    "v_readfirstlane_b32 s53, v205\n\t" \
    "s_cmp_eq_u32 s52, SENT_\n\t" \
    "s_cbranch_scc1 LCH_pf%=\n\t" \
    "LCH_pok%=:\n\t" \
    "s_and_b32 s61, s52, 0xffff\n\t" \
    "s_lshr_b32 s62, s52, 16\n\t" \
    "s_cmp_lt_i32 s53, NPROD_\n\t" \
    "s_cbranch_scc1 LCH_cf%=\n\t" \
    "LCH_cok%=:\n\t" \
    "s_mov_b32 s86, s59\n\t" \
    "s_mov_b32 s87, s60\n\t" \
    "s_branch LCH_loop%=\n\t" \
    "LCH_epi%=:\n\t" \
    "s_mul_i32 s89, s45, 229376\n\t" \
    "ds_read_b128 v[88:91], %[mrd] offset:0\n\t" \
    "ds_read_b128 v[92:95], %[mrd] offset:1024\n\t" \
    "ds_read_b128 v[96:99], %[mrd] offset:2048\n\t" \
    "ds_read_b128 v[100:103], %[mrd] offset:3072\n\t" \
    "ds_read_b128 v[104:107], %[mrd] offset:4096\n\t" \
    "ds_read_b128 v[108:111], %[mrd] offset:5120\n\t" \
    "ds_read_b128 v[112:115], %[mrd] offset:6144\n\t" \
    "s_waitcnt lgkmcnt(6)\n\t" \
    "v_smfmac_f32_16x16x64_bf16 v[140:143], v[88:91], a[0:7], %[sidx]\n\t" \
    "v_smfmac_f32_16x16x64_bf16 v[144:147], v[88:91], a[56:63], %[sidx]\n\t" \
    "v_smfmac_f32_16x16x64_bf16 v[148:151], v[88:91], a[112:119], %[sidx]\n\t" \
    "v_smfmac_f32_16x16x64_bf16 v[152:155], v[88:91], a[168:175], %[sidx]\n\t" \
    "s_waitcnt lgkmcnt(5)\n\t" \
    "v_smfmac_f32_16x16x64_bf16 v[140:143], v[92:95], a[8:15], %[sidx]\n\t" \
    "v_smfmac_f32_16x16x64_bf16 v[144:147], v[92:95], a[64:71], %[sidx]\n\t" \
    "v_smfmac_f32_16x16x64_bf16 v[148:151], v[92:95], a[120:127], %[sidx]\n\t" \
    "v_smfmac_f32_16x16x64_bf16 v[152:155], v[92:95], a[176:183], %[sidx]\n\t" \
    "s_waitcnt lgkmcnt(4)\n\t" \
    "v_smfmac_f32_16x16x64_bf16 v[140:143], v[96:99], a[16:23], %[sidx]\n\t" \
    "v_smfmac_f32_16x16x64_bf16 v[144:147], v[96:99], a[72:79], %[sidx]\n\t" \
    "v_smfmac_f32_16x16x64_bf16 v[148:151], v[96:99], a[128:135], %[sidx]\n\t" \
    "v_smfmac_f32_16x16x64_bf16 v[152:155], v[96:99], a[184:191], %[sidx]\n\t" \
    "s_waitcnt lgkmcnt(3)\n\t" \
    "v_smfmac_f32_16x16x64_bf16 v[140:143], v[100:103], a[24:31], %[sidx]\n\t" \
    "v_smfmac_f32_16x16x64_bf16 v[144:147], v[100:103], a[80:87], %[sidx]\n\t" \
    "v_smfmac_f32_16x16x64_bf16 v[148:151], v[100:103], a[136:143], %[sidx]\n\t" \
    "v_smfmac_f32_16x16x64_bf16 v[152:155], v[100:103], a[192:199], %[sidx]\n\t" \
    "s_waitcnt lgkmcnt(2)\n\t" \
    "v_smfmac_f32_16x16x64_bf16 v[140:143], v[104:107], a[32:39], %[sidx]\n\t" \
    "v_smfmac_f32_16x16x64_bf16 v[144:147], v[104:107], a[88:95], %[sidx]\n\t" \
    "v_smfmac_f32_16x16x64_bf16 v[148:151], v[104:107], a[144:151], %[sidx]\n\t" \
    "v_smfmac_f32_16x16x64_bf16 v[152:155], v[104:107], a[200:207], %[sidx]\n\t" \
    "s_waitcnt lgkmcnt(1)\n\t" \
    "v_smfmac_f32_16x16x64_bf16 v[140:143], v[108:111], a[40:47], %[sidx]\n\t" \
    "v_smfmac_f32_16x16x64_bf16 v[144:147], v[108:111], a[96:103], %[sidx]\n\t" \
    "v_smfmac_f32_16x16x64_bf16 v[148:151], v[108:111], a[152:159], %[sidx]\n\t" \
    "v_smfmac_f32_16x16x64_bf16 v[152:155], v[108:111], a[208:215], %[sidx]\n\t" \
    "s_waitcnt lgkmcnt(0)\n\t" \
    "v_smfmac_f32_16x16x64_bf16 v[140:143], v[112:115], a[48:55], %[sidx]\n\t" \
    "v_smfmac_f32_16x16x64_bf16 v[144:147], v[112:115], a[104:111], %[sidx]\n\t" \
    "v_smfmac_f32_16x16x64_bf16 v[148:151], v[112:115], a[160:167], %[sidx]\n\t" \
    "v_smfmac_f32_16x16x64_bf16 v[152:155], v[112:115], a[216:223], %[sidx]\n\t" \
    "s_nop 6\n\t" \
    "v_cvt_pk_bf16_f32 v156, v140, v141\n\t" \
    "v_cvt_pk_bf16_f32 v157, v142, v143\n\t" \
    "v_cvt_pk_bf16_f32 v158, v144, v145\n\t" \
    "v_cvt_pk_bf16_f32 v159, v146, v147\n\t" \
    "v_cvt_pk_bf16_f32 v160, v148, v149\n\t" \
    "v_cvt_pk_bf16_f32 v161, v150, v151\n\t" \
    "v_cvt_pk_bf16_f32 v162, v152, v153\n\t" \
    "v_cvt_pk_bf16_f32 v163, v154, v155\n\t" \
    "buffer_store_dwordx4 v[156:159], %[owst], %[rsws], s89 offen sc1\n\t" \
    "buffer_store_dwordx4 v[160:163], %[owst], %[rsws], s89 offen offset:1024 sc1\n\t" \
    "v_mov_b32 v199, 1\n\t" \
    "s_waitcnt vmcnt(0)\n\t" \
    "s_barrier\n\t" \
    "s_cmp_lg_u32 %[wv], 0\n\t" \
    "s_cbranch_scc1 LCH_end%=\n\t" \
    "s_mov_b64 s[56:57], exec\n\t" \
    "s_mov_b64 exec, 1\n\t" \
    "buffer_atomic_add v199, %[ofin], %[rsws], 0 offen\n\t" \
    "s_mov_b64 exec, s[56:57]\n\t" \
    "s_branch LCH_end%=\n\t" \
    "LCH_pf%=:\n\t" \
    "s_lshl_b32 s86, s45, 6\n\t" \
    "buffer_load_dword v197, %[opar], %[rsho], s86 offen sc1\n\t" \
    "s_waitcnt vmcnt(0)\n\t" \
    "v_readfirstlane_b32 s52, v197\n\t" \
    "s_add_u32 s49, s49, 1\n\t" \
    "s_cmp_gt_u32 s49, SPIN2_\n\t" \
    "s_cbranch_scc1 LCH_end%=\n\t" \
    "s_cmp_eq_u32 s52, SENT_\n\t" \
    "s_cbranch_scc1 LCH_pf%=\n\t" \
    "s_mov_b32 s49, 0\n\t" \
    "s_branch LCH_pok%=\n\t" \
    "LCH_cf%=:\n\t" \
    "s_lshl_b32 s87, s45, 6\n\t" \
    "s_mov_b32 s49, 0\n\t" \
    "LCH_c1%=:\n\t" \
    "buffer_load_dword v198, %[ocnt], %[rsho], s87 offen sc1\n\t" \
    "s_waitcnt vmcnt(0)\n\t" \
    "v_readfirstlane_b32 s52, v198\n\t" \
    "s_add_u32 s49, s49, 1\n\t" \
    "s_cmp_gt_u32 s49, SPIN2_\n\t" \
    "s_cbranch_scc1 LCH_end%=\n\t" \
    "s_cmp_lt_i32 s52, NPROD_\n\t" \
    "s_cbranch_scc1 LCH_c1%=\n\t" \
    "s_mov_b32 s49, 0\n\t" \
    "s_branch LCH_cok%=\n\t" \
    "LCH_sk1%=:\n\t" \
    "ds_read_b32 v196, %[nadr]\n\t" \
    "buffer_store_dwordx4 a[240:243], %[osst], %[rsho], s88 offen sc1\n\t" \
    "s_cmp_eq_u32 %[ldr], 0\n\t" \
    "s_cbranch_scc1 LCH_bk1%=\n\t" \
    "v_and_b32 v202, 0xff, v200\n\t" \
    "v_and_b32 v203, 0xff, v201\n\t" \
    "v_bfe_u32 v200, v200, 8, 8\n\t" \
    "v_bfe_u32 v201, v201, 8, 8\n\t" \
    "v_max_u32 v202, v202, v203\n\t" \
    "v_max_u32 v203, v200, v201\n\t" \
    "s_nop 0\n\t" \
    "v_max_u32_dpp v202, v202, v202 quad_perm:[1,0,3,2] row_mask:0xf bank_mask:0xf\n\t" \
    "v_max_u32_dpp v203, v203, v203 quad_perm:[1,0,3,2] row_mask:0xf bank_mask:0xf\n\t" \
    "s_nop 0\n\t" \
    "v_max_u32_dpp v202, v202, v202 quad_perm:[2,3,0,1] row_mask:0xf bank_mask:0xf\n\t" \
    "v_max_u32_dpp v203, v203, v203 quad_perm:[2,3,0,1] row_mask:0xf bank_mask:0xf\n\t" \
    "s_nop 0\n\t" \
    "v_max_u32_dpp v202, v202, v202 row_half_mirror row_mask:0xf bank_mask:0xf\n\t" \
    "v_max_u32_dpp v203, v203, v203 row_half_mirror row_mask:0xf bank_mask:0xf\n\t" \
    "s_nop 0\n\t" \
    "v_max_u32_dpp v202, v202, v202 row_mirror row_mask:0xf bank_mask:0xf\n\t" \
    "v_max_u32_dpp v203, v203, v203 row_mirror row_mask:0xf bank_mask:0xf\n\t" \
    "buffer_load_dword v200, %[osl0], %[rsho], s91 offen sc1\n\t" \
    "buffer_load_dword v201, %[osl1], %[rsho], s91 offen sc1\n\t" \
    "s_cmp_lt_u32 s45, FS_\n\t" \
    "s_cselect_b32 s91, s91, s42\n\t" \
    "v_readlane_b32 s52, v202, 0\n\t" \
    "v_readlane_b32 s53, v202, 16\n\t" \
    "v_readlane_b32 s56, v202, 32\n\t" \
    "v_readlane_b32 s57, v202, 48\n\t" \
    "s_max_u32 s52, s52, s53\n\t" \
    "s_max_u32 s56, s56, s57\n\t" \
    "s_max_u32 s52, s52, s56\n\t" \
    "s_add_u32 s53, s63, 24\n\t" \
    "s_cmp_gt_u32 s52, s53\n\t" \
    "s_cselect_b32 s52, 0, s52\n\t" \
    "s_add_u32 s53, s63, OUT_\n\t" \
    "s_min_u32 s52, s52, s53\n\t" \
    "s_min_u32 s53, s52, s92\n\t" \
    "s_cmp_lt_u32 s52, 32\n\t" \
    "s_cselect_b32 s53, 0, s53\n\t" \
    "s_cselect_b32 s92, s92, s52\n\t" \
    "s_and_b32 s56, s45, DEC_\n\t" \
    "s_cmp_eq_u32 s56, 0\n\t" \
    "s_cselect_b32 s95, s94, s95\n\t" \
    "s_cselect_b32 s94, 0, s94\n\t" \
    "s_max_u32 s94, s94, s53\n\t" \
    "s_max_u32 s53, s94, s95\n\t" \
    "s_cmp_lg_u32 s53, 0\n\t" \
    "s_cselect_b32 s63, s53, s63\n\t" \
    "v_readlane_b32 s52, v203, 0\n\t" \
    "v_readlane_b32 s53, v203, 16\n\t" \
    "v_readlane_b32 s56, v203, 32\n\t" \
    "v_readlane_b32 s57, v203, 48\n\t" \
    "s_max_u32 s52, s52, s53\n\t" \
    "s_max_u32 s56, s56, s57\n\t" \
    "s_max_u32 s52, s52, s56\n\t" \
    "s_add_u32 s53, s81, 24\n\t" \
    "s_cmp_gt_u32 s52, s53\n\t" \
    "s_cselect_b32 s52, 0, s52\n\t" \
    "s_add_u32 s53, s81, OUT_\n\t" \
    "s_min_u32 s52, s52, s53\n\t" \
    "s_min_u32 s53, s52, s93\n\t" \
    "s_cmp_lt_u32 s52, 32\n\t" \
    "s_cselect_b32 s53, 0, s53\n\t" \
    "s_cselect_b32 s93, s93, s52\n\t" \
    "s_and_b32 s56, s45, DEC_\n\t" \
    "s_cmp_eq_u32 s56, 0\n\t" \
    "s_cselect_b32 s97, s96, s97\n\t" \
    "s_cselect_b32 s96, 0, s96\n\t" \
    "s_max_u32 s96, s96, s53\n\t" \
    "s_max_u32 s53, s96, s97\n\t" \
    "s_cmp_lg_u32 s53, 0\n\t" \
    "s_cselect_b32 s81, s53, s81\n\t" \
    "s_lshl_b32 s52, s81, 16\n\t" \
    "s_or_b32 s52, s52, s63\n\t" \
    "s_add_u32 s52, s52, TAU1_+(TAU2_<<16)\n\t" \
    "v_mov_b32 v199, s52\n\t" \
    "buffer_store_dword v199, %[opst], %[rsho], s86 offen offset:64 sc1\n\t" \
    "s_branch LCH_bk1%=\n\t" \
    "LCH_sk3%=:\n\t" \
    "ds_read_b32 v196, %[nadr]\n\t" \
    "buffer_store_dwordx4 a[240:243], %[omra], %[rsho], s42 offen sc1\n\t" \
    "s_branch LCH_bk3%=\n\t" \
    "LCH_wst%=:\n\t" \
    "s_cmp_eq_u32 %[wv], 3\n\t" \
    "s_cbranch_scc1 LCH_wst3%=\n\t" \
    "ds_read_b128 v[176:179], %[redr] offset:0\n\t" \
    "ds_read_b128 v[180:183], %[redr] offset:16\n\t" \
    "ds_read_b128 v[184:187], %[redr] offset:32\n\t" \
    "ds_read_b128 v[188:191], %[redr] offset:48\n\t" \
    "ds_read_b32 v196, %[nadr]\n\t" \
    "s_waitcnt lgkmcnt(3)\n\t" \
    "v_pk_add_f32 v[176:177], v[176:177], v[180:181]\n\t" \
    "v_pk_add_f32 v[178:179], v[178:179], v[182:183]\n\t" \
    "s_waitcnt lgkmcnt(1)\n\t" \
    "v_pk_add_f32 v[184:185], v[184:185], v[188:189]\n\t" \
    "v_pk_add_f32 v[186:187], v[186:187], v[190:191]\n\t" \
    "v_pk_add_f32 v[176:177], v[176:177], v[184:185]\n\t" \
    "v_pk_add_f32 v[178:179], v[178:179], v[186:187]\n\t" \
    "v_cvt_pk_bf16_f32 v192, v176, v177\n\t" \
    "v_cvt_pk_bf16_f32 v193, v178, v179\n\t" \
    "s_nop 0\n\t" \
    "v_mov_b32_dpp v194, v192 row_shl:4 row_mask:0xf bank_mask:0xf\n\t" \
    "v_mov_b32_dpp v195, v193 row_shl:4 row_mask:0xf bank_mask:0xf\n\t" \
    "buffer_store_dwordx4 v[192:195], %[omst], %[rsho], s99 offen sc1\n\t" \
    "s_waitcnt vmcnt(7) lgkmcnt(0)\n\t" \
    "s_branch LCH_Ago%=\n\t" \
    "LCH_wst3%=:\n\t" \
    "buffer_store_dwordx4 v[156:159], %[owst], %[rsws], s89 offen sc1\n\t" \
    "buffer_store_dwordx4 v[160:163], %[owst], %[rsws], s89 offen offset:1024 sc1\n\t" \
    "s_waitcnt vmcnt(8)\n\t" \
    "LCH_Ago%=:\n\t" \
    "v_and_b32 v72, %[mlo], v64\n\t" \
    "v_and_b32 v73, %[mlo], v65\n\t" \
    "v_and_b32 v74, %[mlo], v66\n\t" \
    "v_and_b32 v75, %[mlo], v67\n\t" \
    "v_and_b32 v76, %[mhi], v64\n\t" \
    "v_and_b32 v77, %[mhi], v65\n\t" \
    "v_mfma_f32_16x16x32_bf16 v[88:91], v[32:35], v[72:75], 0\n\t" \
    "v_and_b32 v78, %[mhi], v66\n\t" \
    "v_and_b32 v79, %[mhi], v67\n\t" \
    "v_mfma_f32_16x16x32_bf16 v[92:95], v[40:43], v[72:75], 0\n\t" \
    "s_nop 0\n\t" \
    "v_mfma_f32_16x16x32_bf16 v[88:91], v[36:39], v[76:79], v[88:91]\n\t" \
    "s_branch LCH_Ago2%=\n\t" \
    "LCH_A%=_slip:\n\t" \
    "s_add_u32 s40, s40, QS_\n\t" \
    "s_add_u32 s41, s41, 1\n\t" \
    "v_mov_b32 v191, s41\n\t" \
    "ds_max_i32 %[nadr], v191\n\t" \
    "s_branch LCH_A%=_w0\n\t" \
    "LCH_C%=_slip:\n\t" \
    "s_add_u32 s40, s40, QS_\n\t" \
    "s_add_u32 s41, s41, 1\n\t" \
    "v_mov_b32 v191, s41\n\t" \
    "ds_max_i32 %[nadr], v191\n\t" \
    "s_branch LCH_C%=_w0\n\t" \
    "LCH_Afail%=:\n\t" \
    "s_cmp_eq_u32 s45, 0\n\t" \
    "s_cbranch_scc1 LCH_Aok%=\n\t" \
    "s_mov_b32 s52, SENT_\n\t" \
    "s_mov_b64 s[56:57], 0\n\t" \
    "v_cmp_eq_u32 vcc, s52, v64\n\t" \
    "s_or_b64 s[56:57], s[56:57], vcc\n\t" \
    "v_cmp_eq_u32 vcc, s52, v65\n\t" \
    "s_or_b64 s[56:57], s[56:57], vcc\n\t" \
    "v_cmp_eq_u32 vcc, s52, v66\n\t" \
    "s_or_b64 s[56:57], s[56:57], vcc\n\t" \
    "v_cmp_eq_u32 vcc, s52, v67\n\t" \
    "s_or_b64 s[56:57], s[56:57], vcc\n\t" \
    "v_cmp_eq_u32 vcc, s52, v68\n\t" \
    "s_or_b64 s[56:57], s[56:57], vcc\n\t" \
    "v_cmp_eq_u32 vcc, s52, v69\n\t" \
    "s_or_b64 s[56:57], s[56:57], vcc\n\t" \
    "v_cmp_eq_u32 vcc, s52, v70\n\t" \
    "s_or_b64 s[56:57], s[56:57], vcc\n\t" \
    "v_cmp_eq_u32 vcc, s52, v71\n\t" \
    "s_or_b64 s[56:57], s[56:57], vcc\n\t" \
    "s_cmp_eq_u64 s[56:57], 0\n\t" \
    "s_cbranch_scc1 LCH_Aok%=\n\t" \
    "s_add_u32 s49, s49, 1\n\t" \
    "s_cmp_gt_u32 s49, SPIN_\n\t" \
    "s_cbranch_scc1 LCH_end%=\n\t" \
    "LCH_Af%=_r0:\n\t" \
    "s_add_u32 s40, s40, QS_\n\t" \
    "s_add_u32 s41, s41, 1\n\t" \
    "s_memrealtime s[54:55]\n\t" \
    "s_waitcnt lgkmcnt(0)\n\t" \
    "s_sub_u32 s48, s54, s40\n\t" \
    "s_cmp_gt_i32 s48, TOLR_\n\t" \
    "s_cbranch_scc1 LCH_Af%=_r0\n\t" \
    "v_mov_b32 v191, s41\n\t" \
    "ds_max_i32 %[nadr], v191\n\t" \
    "s_bitcmp1_b32 s41, 0\n\t" \
    "s_cselect_b32 s44, HO_COPY_, 0\n\t" \
    "s_add_u32 s44, s44, s42\n\t" \
    "LCH_Af%=_r1:\n\t" \
    "s_memrealtime s[54:55]\n\t" \
    "s_waitcnt lgkmcnt(0)\n\t" \
    "s_sub_u32 s48, s54, s40\n\t" \
    "s_cmp_lt_i32 s48, 0\n\t" \
    "s_cbranch_scc1 LCH_Af%=_r1\n\t" \
    "buffer_load_dwordx4 v[64:67], %[ora], %[rsho], s44 offen sc1\n\t" \
    "buffer_load_dwordx4 v[68:71], %[orb], %[rsho], s44 offen sc1\n\t" \
    "s_lshl_b32 s53, s42, 1\n\t" \
    "s_add_u32 s53, s53, HO_COPY_\n\t" \
    "s_sub_u32 s98, s53, s44\n\t" \
    "s_waitcnt vmcnt(0)\n\t" \
    "s_branch LCH_Ago%=\n\t" \
    "LCH_Cfail%=:\n\t" \
    "s_mov_b32 s52, SENT_\n\t" \
    "s_mov_b64 s[56:57], 0\n\t" \
    "v_cmp_eq_u32 vcc, s52, v164\n\t" \
    "s_or_b64 s[56:57], s[56:57], vcc\n\t" \
    "v_cmp_eq_u32 vcc, s52, v165\n\t" \
    "s_or_b64 s[56:57], s[56:57], vcc\n\t" \
    "v_cmp_eq_u32 vcc, s52, v166\n\t" \
    "s_or_b64 s[56:57], s[56:57], vcc\n\t" \
    "v_cmp_eq_u32 vcc, s52, v167\n\t" \
    "s_or_b64 s[56:57], s[56:57], vcc\n\t" \
    "v_cmp_eq_u32 vcc, s52, v168\n\t" \
    "s_or_b64 s[56:57], s[56:57], vcc\n\t" \
    "v_cmp_eq_u32 vcc, s52, v169\n\t" \
    "s_or_b64 s[56:57], s[56:57], vcc\n\t" \
    "v_cmp_eq_u32 vcc, s52, v170\n\t" \
    "s_or_b64 s[56:57], s[56:57], vcc\n\t" \
    "v_cmp_eq_u32 vcc, s52, v172\n\t" \
    "s_or_b64 s[56:57], s[56:57], vcc\n\t" \
    "v_cmp_eq_u32 vcc, s52, v173\n\t" \
    "s_or_b64 s[56:57], s[56:57], vcc\n\t" \
    "v_cmp_eq_u32 vcc, s52, v174\n\t" \
    "s_or_b64 s[56:57], s[56:57], vcc\n\t" \
    "v_cmp_eq_u32 vcc, s52, v175\n\t" \
    "s_or_b64 s[56:57], s[56:57], vcc\n\t" \
    "s_cmp_eq_u64 s[56:57], 0\n\t" \
    "s_cbranch_scc1 LCH_Cok%=\n\t" \
    "s_add_u32 s49, s49, 1\n\t" \
    "s_cmp_gt_u32 s49, SPIN_\n\t" \
    "s_cbranch_scc1 LCH_end%=\n\t" \
    "LCH_Cf%=_r0:\n\t" \
    "s_add_u32 s40, s40, QS_\n\t" \
    "s_add_u32 s41, s41, 1\n\t" \
    "s_memrealtime s[54:55]\n\t" \
    "s_waitcnt lgkmcnt(0)\n\t" \
    "s_sub_u32 s48, s54, s40\n\t" \
    "s_cmp_gt_i32 s48, TOLR_\n\t" \
    "s_cbranch_scc1 LCH_Cf%=_r0\n\t" \
    "v_mov_b32 v191, s41\n\t" \
    "ds_max_i32 %[nadr], v191\n\t" \
    "s_bitcmp1_b32 s41, 0\n\t" \
    "s_cselect_b32 s44, HO_COPY_, 0\n\t" \
    "s_add_u32 s44, s44, s42\n\t" \
    "LCH_Cf%=_r1:\n\t" \
    "s_memrealtime s[54:55]\n\t" \
    "s_waitcnt lgkmcnt(0)\n\t" \
    "s_sub_u32 s48, s54, s40\n\t" \
    "s_cmp_lt_i32 s48, 0\n\t" \
    "s_cbranch_scc1 LCH_Cf%=_r1\n\t" \
    "buffer_load_dword v164, %[osmm], %[rsho], s44 offen offset:0 sc1\n\t" \
    "buffer_load_dword v165, %[osmm], %[rsho], s44 offen offset:64 sc1\n\t" \
    "buffer_load_dword v166, %[osmm], %[rsho], s44 offen offset:128 sc1\n\t" \
    "buffer_load_dword v167, %[osmm], %[rsho], s44 offen offset:192 sc1\n\t" \
    "buffer_load_dword v168, %[osmm], %[rsho], s44 offen offset:256 sc1\n\t" \
    "buffer_load_dword v169, %[osmm], %[rsho], s44 offen offset:320 sc1\n\t" \
    "buffer_load_dword v170, %[osmm], %[rsho], s44 offen offset:384 sc1\n\t" \
    "buffer_load_dwordx4 v[172:175], %[osx], %[rsho], s44 offen sc1\n\t" \
    "s_waitcnt vmcnt(0)\n\t" \
    "s_branch LCH_Cgo%=\n\t" \
    "LCH_end%=:\n\t" \
    "s_waitcnt vmcnt(0) lgkmcnt(0)\n\t" \
    ""
#define CHAIN_G_ASM \
    "s_mov_b32 s50, 0\n\t" \
    "s_mov_b32 s87, 0\n\t" \
    "s_mov_b32 s61, C1_+C2_\n\t" \
    "s_mov_b32 s63, 0\n\t" \
    "s_mov_b32 s49, 0\n\t" \
    "LG_c0%=:\n\t" \
    "buffer_load_dword v198, %[ocnt], %[rsho], s87 offen sc1\n\t" \
    "s_waitcnt vmcnt(0)\n\t" \
    "v_readfirstlane_b32 s52, v198\n\t" \
    "s_add_u32 s49, s49, 1\n\t" \
    "s_cmp_gt_u32 s49, SPIN2_\n\t" \
    "s_cbranch_scc1 LG_end%=\n\t" \
    "s_cmp_lt_i32 s52, NPROD_\n\t" \
    "s_cbranch_scc1 LG_c0%=\n\t" \
    "s_mov_b32 s49, 0\n\t" \
    "s_lshr_b32 s52, s50, 1\n\t" \
    "s_xor_b32 s52, s52, %[par]\n\t" \
    "s_and_b32 s52, s52, 1\n\t" \
    "s_xor_b32 s52, s52, FAR_\n\t" \
    "s_lshl_b32 s52, s52, 13\n\t" \
    "s_lshl_b32 s53, s50, 20\n\t" \
    "s_add_u32 s52, s52, s53\n\t" \
    "s_add_u32 s53, s52, 32768\n\t" \
    "buffer_load_dwordx4 v[0:3], %[og0], %[rsho], s52 offen offset:0 sc1\n\t" \
    "buffer_load_dwordx4 v[16:19], %[og0], %[rsho], s53 offen offset:0 sc1\n\t" \
    "buffer_load_dwordx4 v[8:11], %[og1], %[rsho], s52 offen offset:0 sc1\n\t" \
    "buffer_load_dwordx4 v[24:27], %[og1], %[rsho], s53 offen offset:0 sc1\n\t" \
    "buffer_load_dwordx4 v[4:7], %[og0], %[rsho], s52 offen offset:1024 sc1\n\t" \
    "buffer_load_dwordx4 v[20:23], %[og0], %[rsho], s53 offen offset:1024 sc1\n\t" \
    "buffer_load_dwordx4 v[12:15], %[og1], %[rsho], s52 offen offset:1024 sc1\n\t" \
    "buffer_load_dwordx4 v[28:31], %[og1], %[rsho], s53 offen offset:1024 sc1\n\t" \
    "s_add_u32 s50, s50, 1\n\t" \
    "s_mov_b32 s45, 0\n\t" \
    "s_mov_b32 s46, 0\n\t" \
    "s_mov_b32 s47, 0\n\t" \
    "s_mov_b32 s49, 0\n\t" \
    "s_and_b32 s42, %[par], 1\n\t" \
    "s_lshl_b32 s42, s42, 13\n\t" \
    "s_mov_b32 s46, s42\n\t" \
    "s_mov_b32 s88, 0x40000000\n\t" \
    "v_mov_b32 v199, SENT_\n\t" \
    "v_accvgpr_write_b32 a240, v199\n\t" \
    "v_accvgpr_write_b32 a241, v199\n\t" \
    "v_accvgpr_write_b32 a242, v199\n\t" \
    "v_accvgpr_write_b32 a243, v199\n\t" \
    "s_waitcnt vmcnt(0)\n\t" \
    "s_memrealtime s[54:55]\n\t" \
    "s_waitcnt lgkmcnt(0)\n\t" \
    "s_lshr_b32 s41, s54, QS_LOG\n\t" \
    "s_add_u32 s41, s41, 1\n\t" \
    "s_lshl_b32 s40, s41, QS_LOG\n\t" \
    ".p2align 6\n\t" \
    "LG_loop%=:\n\t" \
    "s_memrealtime s[54:55]\n\t" \
    "s_bitcmp1_b32 s41, 0\n\t" \
    "s_cselect_b32 s44, HO_COPY_, 0\n\t" \
    "s_add_u32 s44, s44, s42\n\t" \
    "s_waitcnt lgkmcnt(0)\n\t" \
    "s_sub_u32 s48, s54, s40\n\t" \
    "s_cmp_gt_i32 s48, TOL_\n\t" \
    "s_cbranch_scc1 LG_slip%=\n\t" \
    "LG_w1%=:\n\t" \
    "s_memrealtime s[54:55]\n\t" \
    "s_waitcnt lgkmcnt(0)\n\t" \
    "s_sub_u32 s48, s54, s40\n\t" \
    "s_cmp_lt_i32 s48, -PH_\n\t" \
    "s_cbranch_scc1 LG_w1%=\n\t" \
    "LG_w2%=:\n\t" \
    "s_add_i32 s48, s48, 1\n\t" \
    "s_cmp_le_i32 s48, 0\n\t" \
    "s_cbranch_scc1 LG_w2%=\n\t" \
    "buffer_load_dwordx4 v[64:67], %[ora], %[rsho], s44 offen sc1\n\t" \
    "buffer_load_dwordx4 v[68:71], %[orb], %[rsho], s44 offen sc1\n\t" \
    "s_lshl_b32 s53, s42, 1\n\t" \
    "s_add_u32 s53, s53, HO_COPY_\n\t" \
    "s_sub_u32 s51, s53, s44\n\t" \
    "s_waitcnt vmcnt(1)\n\t" \
    "v_and_b32 v72, %[mlo], v64\n\t" \
    "v_and_b32 v73, %[mlo], v65\n\t" \
    "v_and_b32 v74, %[mlo], v66\n\t" \
    "v_and_b32 v75, %[mlo], v67\n\t" \
    "v_and_b32 v76, %[mhi], v64\n\t" \
    "v_and_b32 v77, %[mhi], v65\n\t" \
    "v_and_b32 v78, %[mhi], v66\n\t" \
    "v_and_b32 v79, %[mhi], v67\n\t" \
    "v_mfma_f32_16x16x32_bf16 v[88:91], v[0:3], v[72:75], 0\n\t" \
    "s_waitcnt vmcnt(0)\n\t" \
    "v_and_b32 v80, %[mlo2], v68\n\t" \
    "v_and_b32 v81, %[mlo2], v69\n\t" \
    "v_mfma_f32_16x16x32_bf16 v[92:95], v[16:19], v[72:75], 0\n\t" \
    "v_and_b32 v82, %[mlo2], v70\n\t" \
    "v_and_b32 v83, %[mlo2], v71\n\t" \
    "v_mfma_f32_16x16x32_bf16 v[88:91], v[4:7], v[76:79], v[88:91]\n\t" \
    "v_and_b32 v84, %[mhi2], v68\n\t" \
    "v_and_b32 v85, %[mhi2], v69\n\t" \
    "v_mfma_f32_16x16x32_bf16 v[92:95], v[20:23], v[76:79], v[92:95]\n\t" \
    "v_and_b32 v86, %[mhi2], v70\n\t" \
    "v_and_b32 v87, %[mhi2], v71\n\t" \
    "v_mfma_f32_16x16x32_bf16 v[88:91], v[8:11], v[80:83], v[88:91]\n\t" \
    "v_mfma_f32_16x16x32_bf16 v[92:95], v[24:27], v[80:83], v[92:95]\n\t" \
    "v_mfma_f32_16x16x32_bf16 v[88:91], v[12:15], v[84:87], v[88:91]\n\t" \
    "v_mfma_f32_16x16x32_bf16 v[92:95], v[28:31], v[84:87], v[92:95]\n\t" \
    "s_nop 3\n\t" \
    "ds_write_b128 %[pw], v[88:91]\n\t" \
    "s_nop 2\n\t" \
    "ds_write_b128 %[pw2], v[92:95]\n\t" \
    "v_cmp_u_f32 vcc, v88, v92\n\t" \
    "s_cbranch_vccnz LG_fail%=\n\t" \
    "LG_ok%=:\n\t" \
    "s_waitcnt lgkmcnt(0)\n\t" \
    "s_barrier\n\t" \
    "ds_read2st64_b32 v[176:177], %[prd] offset0:0 offset1:4\n\t" \
    "ds_read2st64_b32 v[178:179], %[prd] offset0:8 offset1:12\n\t" \
    "ds_read2st64_b32 v[180:181], %[prd] offset0:16 offset1:20\n\t" \
    "ds_read2st64_b32 v[182:183], %[prd] offset0:24 offset1:28\n\t" \
    "s_waitcnt lgkmcnt(2)\n\t" \
    "v_pk_add_f32 v[176:177], v[176:177], v[178:179]\n\t" \
    "s_waitcnt lgkmcnt(0)\n\t" \
    "v_pk_add_f32 v[180:181], v[180:181], v[182:183]\n\t" \
    "v_pk_add_f32 v[176:177], v[176:177], v[180:181]\n\t" \
    "v_add_f32 v192, v176, v177\n\t" \
    "s_nop 1\n\t" \
    "v_mov_b32_dpp v193, v192 row_shl:4 row_mask:0xf bank_mask:0xf\n\t" \
    "v_mov_b32_dpp v194, v192 row_shl:8 row_mask:0xf bank_mask:0xf\n\t" \
    "v_mov_b32_dpp v195, v192 row_shl:12 row_mask:0xf bank_mask:0xf\n\t" \
    "buffer_store_dwordx4 v[192:195], %[oxst], %[rsho], s44 offen sc1\n\t" \
    "s_add_u32 s86, s45, 1\n\t" \
    "s_lshl_b32 s87, s86, 6\n\t" \
    "s_lshl_b32 s86, s86, 6\n\t" \
    "buffer_store_dwordx4 v[192:195], %[oxst], %[rsho], s51 offen sc1\n\t" \
    "s_add_u32 s52, s88, HO_COPY_\n\t" \
    "buffer_store_dwordx4 a[240:243], %[oxst], %[rsho], s88 offen sc1\n\t" \
    "buffer_store_dwordx4 a[240:243], %[oxst], %[rsho], s52 offen sc1\n\t" \
    "LG_pl%=:\n\t" \
    "buffer_load_dword v197, %[opar], %[rsho], s86 offen sc1\n\t" \
    "s_mov_b32 s49, 0\n\t" \
    "LG_c1%=:\n\t" \
    "buffer_load_dword v198, %[ocnt], %[rsho], s87 offen sc1\n\t" \
    "s_waitcnt vmcnt(0)\n\t" \
    "v_readfirstlane_b32 s52, v198\n\t" \
    "s_add_u32 s49, s49, 1\n\t" \
    "s_cmp_gt_u32 s49, SPIN2_\n\t" \
    "s_cbranch_scc1 LG_end%=\n\t" \
    "s_cmp_lt_i32 s52, NPROD_\n\t" \
    "s_cbranch_scc1 LG_c1%=\n\t" \
    "s_mov_b32 s49, 0\n\t" \
    "v_readfirstlane_b32 s62, v197\n\t" \
    "s_add_u32 s63, s63, 1\n\t" \
    "s_cmp_gt_u32 s63, SPIN2_\n\t" \
    "s_cbranch_scc1 LG_end%=\n\t" \
    "s_cmp_eq_u32 s62, SENT_\n\t" \
    "s_cbranch_scc1 LG_pl%=\n\t" \
    "s_lshr_b32 s52, s50, 1\n\t" \
    "s_xor_b32 s52, s52, %[par]\n\t" \
    "s_and_b32 s52, s52, 1\n\t" \
    "s_xor_b32 s52, s52, FAR_\n\t" \
    "s_lshl_b32 s52, s52, 13\n\t" \
    "s_lshl_b32 s53, s50, 20\n\t" \
    "s_add_u32 s52, s52, s53\n\t" \
    "s_add_u32 s53, s52, 32768\n\t" \
    "buffer_load_dwordx4 v[0:3], %[og0], %[rsho], s52 offen offset:0 sc1\n\t" \
    "buffer_load_dwordx4 v[16:19], %[og0], %[rsho], s53 offen offset:0 sc1\n\t" \
    "buffer_load_dwordx4 v[8:11], %[og1], %[rsho], s52 offen offset:0 sc1\n\t" \
    "buffer_load_dwordx4 v[24:27], %[og1], %[rsho], s53 offen offset:0 sc1\n\t" \
    "buffer_load_dwordx4 v[4:7], %[og0], %[rsho], s52 offen offset:1024 sc1\n\t" \
    "buffer_load_dwordx4 v[20:23], %[og0], %[rsho], s53 offen offset:1024 sc1\n\t" \
    "buffer_load_dwordx4 v[12:15], %[og1], %[rsho], s52 offen offset:1024 sc1\n\t" \
    "buffer_load_dwordx4 v[28:31], %[og1], %[rsho], s53 offen offset:1024 sc1\n\t" \
    "s_add_u32 s50, s50, 1\n\t" \
    "ds_read_b32 v181, %[nadr]\n\t" \
    "s_mov_b32 s88, s42\n\t" \
    "s_add_u32 s45, s45, 1\n\t" \
    "s_and_b32 s52, s45, 3\n\t" \
    "s_mul_i32 s42, s52, HO_STEP_\n\t" \
    "s_add_u32 s42, s42, s46\n\t" \
    "s_waitcnt lgkmcnt(0)\n\t" \
    "s_barrier\n\t" \
    "v_readfirstlane_b32 s52, v181\n\t" \
    "s_nop 3\n\t" \
    "s_sub_i32 s52, s52, s41\n\t" \
    "s_max_i32 s52, s52, 0\n\t" \
    "s_add_u32 s41, s41, s52\n\t" \
    "s_lshl_b32 s52, s52, QS_LOG\n\t" \
    "s_add_u32 s40, s40, s52\n\t" \
    "s_add_u32 s40, s40, s61\n\t" \
    "s_and_b32 s61, s62, 0xffff\n\t" \
    "s_lshr_b32 s62, s62, 16\n\t" \
    "s_add_u32 s61, s61, s62\n\t" \
    "s_mov_b32 s49, 0\n\t" \
    "s_cmp_lt_u32 s45, NSTEP_\n\t" \
    "s_cbranch_scc1 LG_loop%=\n\t" \
    "s_branch LG_end%=\n\t" \
    "LG_fail%=:\n\t" \
    "s_cmp_eq_u32 s45, 0\n\t" \
    "s_cbranch_scc1 LG_ok%=\n\t" \
    "s_mov_b32 s52, SENT_\n\t" \
    "s_mov_b64 s[56:57], 0\n\t" \
    "v_cmp_eq_u32 vcc, s52, v64\n\t" \
    "s_or_b64 s[56:57], s[56:57], vcc\n\t" \
    "v_cmp_eq_u32 vcc, s52, v65\n\t" \
    "s_or_b64 s[56:57], s[56:57], vcc\n\t" \
    "v_cmp_eq_u32 vcc, s52, v66\n\t" \
    "s_or_b64 s[56:57], s[56:57], vcc\n\t" \
    "v_cmp_eq_u32 vcc, s52, v67\n\t" \
    "s_or_b64 s[56:57], s[56:57], vcc\n\t" \
    "v_cmp_eq_u32 vcc, s52, v68\n\t" \
    "s_or_b64 s[56:57], s[56:57], vcc\n\t" \
    "v_cmp_eq_u32 vcc, s52, v69\n\t" \
    "s_or_b64 s[56:57], s[56:57], vcc\n\t" \
    "v_cmp_eq_u32 vcc, s52, v70\n\t" \
    "s_or_b64 s[56:57], s[56:57], vcc\n\t" \
    "v_cmp_eq_u32 vcc, s52, v71\n\t" \
    "s_or_b64 s[56:57], s[56:57], vcc\n\t" \
    "s_cmp_eq_u64 s[56:57], 0\n\t" \
    "s_cbranch_scc1 LG_ok%=\n\t" \
    "s_add_u32 s49, s49, 1\n\t" \
    "s_cmp_gt_u32 s49, SPIN_\n\t" \
    "s_cbranch_scc1 LG_end%=\n\t" \
    "LG_slip%=:\n\t" \
    "s_add_u32 s40, s40, QS_\n\t" \
    "s_add_u32 s41, s41, 1\n\t" \
    "v_mov_b32 v191, s41\n\t" \
    "ds_max_i32 %[nadr], v191\n\t" \
    "s_branch LG_loop%=\n\t" \
    "LG_end%=:\n\t" \
    "s_waitcnt vmcnt(0) lgkmcnt(0)\n\t" \
    ""
#define CHAIN_CLOBBERS \
    "v0", "v1", "v2", "v3", "v4", "v5", "v6", "v7", "v8", "v9", "v10", "v11", "v12", "v13", "v14", "v15", "v16", "v17", "v18", "v19", "v20", "v21", "v22", "v23", \
    "v24", "v25", "v26", "v27", "v28", "v29", "v30", "v31", "v32", "v33", "v34", "v35", "v36", "v37", "v38", "v39", "v40", "v41", "v42", "v43", "v44", "v45", "v46", "v47", \
    "v48", "v49", "v50", "v51", "v52", "v53", "v54", "v55", "v56", "v57", "v58", "v59", "v60", "v61", "v62", "v63", "v64", "v65", "v66", "v67", "v68", "v69", "v70", "v71", \
    "v72", "v73", "v74", "v75", "v76", "v77", "v78", "v79", "v80", "v81", "v82", "v83", "v84", "v85", "v86", "v87", "v88", "v89", "v90", "v91", "v92", "v93", "v94", "v95", \
    "v96", "v97", "v98", "v99", "v100", "v101", "v102", "v103", "v104", "v105", "v106", "v107", "v108", "v109", "v110", "v111", "v112", "v113", "v114", "v115", "v116", "v117", "v118", "v119", \
    "v120", "v121", "v122", "v123", "v124", "v125", "v126", "v127", "v128", "v129", "v130", "v131", "v132", "v133", "v134", "v135", "v136", "v137", "v138", "v139", "v140", "v141", "v142", "v143", \
    "v144", "v145", "v146", "v147", "v148", "v149", "v150", "v151", "v152", "v153", "v154", "v155", "v156", "v157", "v158", "v159", "v160", "v161", "v162", "v163", "v164", "v165", "v166", "v167", \
    "v168", "v169", "v170", "v171", "v172", "v173", "v174", "v175", "v176", "v177", "v178", "v179", "v180", "v181", "v182", "v183", "v184", "v185", "v186", "v187", "v188", "v189", "v190", "v191", \
    "v192", "v193", "v194", "v195", "v196", "v197", "v198", "v199", "v200", "v201", "v202", "v203", "v204", "v205", "a0", "a1", "a2", "a3", "a4", "a5", "a6", "a7", "a8", "a9", \
    "a10", "a11", "a12", "a13", "a14", "a15", "a16", "a17", "a18", "a19", "a20", "a21", "a22", "a23", "a24", "a25", "a26", "a27", "a28", "a29", "a30", "a31", "a32", "a33", \
    "a34", "a35", "a36", "a37", "a38", "a39", "a40", "a41", "a42", "a43", "a44", "a45", "a46", "a47", "a48", "a49", "a50", "a51", "a52", "a53", "a54", "a55", "a56", "a57", \
    "a58", "a59", "a60", "a61", "a62", "a63", "a64", "a65", "a66", "a67", "a68", "a69", "a70", "a71", "a72", "a73", "a74", "a75", "a76", "a77", "a78", "a79", "a80", "a81", \
    "a82", "a83", "a84", "a85", "a86", "a87", "a88", "a89", "a90", "a91", "a92", "a93", "a94", "a95", "a96", "a97", "a98", "a99", "a100", "a101", "a102", "a103", "a104", "a105", \
    "a106", "a107", "a108", "a109", "a110", "a111", "a112", "a113", "a114", "a115", "a116", "a117", "a118", "a119", "a120", "a121", "a122", "a123", "a124", "a125", "a126", "a127", "a128", "a129", \
    "a130", "a131", "a132", "a133", "a134", "a135", "a136", "a137", "a138", "a139", "a140", "a141", "a142", "a143", "a144", "a145", "a146", "a147", "a148", "a149", "a150", "a151", "a152", "a153", \
    "a154", "a155", "a156", "a157", "a158", "a159", "a160", "a161", "a162", "a163", "a164", "a165", "a166", "a167", "a168", "a169", "a170", "a171", "a172", "a173", "a174", "a175", "a176", "a177", \
    "a178", "a179", "a180", "a181", "a182", "a183", "a184", "a185", "a186", "a187", "a188", "a189", "a190", "a191", "a192", "a193", "a194", "a195", "a196", "a197", "a198", "a199", "a200", "a201", \
    "a202", "a203", "a204", "a205", "a206", "a207", "a208", "a209", "a210", "a211", "a212", "a213", "a214", "a215", "a216", "a217", "a218", "a219", "a220", "a221", "a222", "a223", "a224", "a225", \
    "a226", "a227", "a228", "a229", "a230", "a231", "a232", "a233", "a234", "a235", "a236", "a237", "a238", "a239", "a240", "a241", "a242", "a243", "a244", "a245", "a246", "a247", "a248", "a249", \
    "a250", "a251", "a252", "a253", "a254", "a255", "s40", "s41", "s42", "s43", "s44", "s45", "s46", "s47", "s48", "s49", "s50", "s51", "s52", "s53", "s54", "s55", "s56", "s57", \
    "s58", "s59", "s60", "s61", "s62", "s63", "s64", "s65", "s66", "s67", "s68", "s69", "s70", "s71", "s72", "s73", "s74", "s75", "s76", "s77", "s78", "s79", "s80", "s81", \
    "s82", "s83", "s84", "s85", "s86", "s87", "s88", "s89", "s90", "s91", "s92", "s93", "s94", "s95", "s96", "s97", "s98", "s99", "vcc", "scc", "memory"

#define STR2(x) #x
#define STR(x) STR2(x)
#define CHAIN_CONSTS ".set QS_LOG, " STR(QS_LOG2) "\n\t.set QS_, (1<<" STR(QS_LOG2) ")\n\t.set TOL_, " STR(TOL_T) "\n\t.set C1_, " STR(C1_T) "\n\t.set C2_, " STR(C2_T) "\n\t" \
    ".set HO_COPY_, 114688\n\t.set HO_STEP_, 229376\n\t.set HO_SPC_, 9\n\t.set NSTEP_, 63\n\t.set SENT_, 0x7F817F81\n\t.set SPIN_, 65536\n\t.set SPIN2_, 200000\n\t.set NPROD_, 56\n\t.set CMAX_, 120\n\t.set FS_, " STR(FS_T) "\n\t.set OUT_, " STR(OUT_T) "\n\t.set TOLR_, " STR(TOLR_T) "\n\t.set DLY_, " STR(DLY_T) "\n\t.set TAU1_, " STR(TAU1_T) "\n\t.set TAU2_, " STR(TAU2_T) "\n\t.set DEC_, " STR(DEC_T) "\n\t.set FAR_, " STR(FAR_T) "\n\t.set PH_, " STR(PH_T) "\n\t.set STEP_, " STR(STEP_T) "\n\t.set SCORE_, " STR(SCORE_T) "\n\t.set QUIET_, " STR(QUIET_T) "\n\t.set HOLD0_, " STR(HOLD0_T) "\n\t.set HOLD_, " STR(HOLD_T) "\n\ts_nop 4\n\t"
static_assert(HO_COPY == 114688 && HO_STEP == 229376 && HO_SPC == 9, "asm constants");

__device__ __forceinline__ int take_slot(unsigned* ctrl, LAS unsigned* lds_word) {
    if (threadIdx.x == 0) {
        const unsigned x = xcc_id();
        unsigned t = 32;
        if (x < 8) t = __hip_atomic_fetch_add(ctrl + 16 * (1 + x), 1u, __ATOMIC_RELAXED, __HIP_MEMORY_SCOPE_AGENT);
        asm volatile("" :: "v"(t) : "memory");
        __hip_atomic_fetch_add(ctrl + 16 * 9, 1u, __ATOMIC_RELAXED, __HIP_MEMORY_SCOPE_AGENT);
        unsigned slot;
        if (t < 32) slot = x * 32 + t;
        else {
            unsigned o = __hip_atomic_fetch_add(ctrl + 16 * 10, 1u, __ATOMIC_RELAXED, __HIP_MEMORY_SCOPE_AGENT);
            unsigned sp = 0;
            while (__hip_atomic_load(ctrl + 16 * 9, __ATOMIC_RELAXED, __HIP_MEMORY_SCOPE_AGENT) < 256u && ++sp < (1u << 22)) __builtin_amdgcn_s_sleep(2);
            slot = 255;
            for (unsigned xx = 0; xx < 8; ++xx) {
                unsigned c = __hip_atomic_load(ctrl + 16 * (1 + xx), __ATOMIC_RELAXED, __HIP_MEMORY_SCOPE_AGENT);
                c = c < 32u ? c : 32u;
                unsigned fr = 32u - c;
                if (o < fr) { slot = xx * 32 + c + o; break; }
                o -= fr;
            }
        }
        *lds_word = slot;
    }
    __syncthreads();
    int r = (int)__builtin_amdgcn_readfirstlane(*lds_word);
    __syncthreads();
    return r;
}

constexpr int CT_Q = 256;
constexpr int CT_KV = 384;
constexpr int CT_BW = 400;
constexpr int CT_P1 = 416;
constexpr int CT_G = 1024;
constexpr int CT_M = 2048;
constexpr int CT_SIG = 512;
constexpr size_t WS_KVF = 52 * MB;
__device__ __forceinline__ int lane_id() { int l = __builtin_amdgcn_mbcnt_hi(~0u, __builtin_amdgcn_mbcnt_lo(~0u, 0u)); asm volatile("" : "+v"(l)); return l; }
__device__ __forceinline__ unsigned* gcnt(const Params& p, unsigned parity) { return (unsigned*)(ho_base(p.ws) + HO_CNT + (parity & 1u) * 8192u); }
#define AGENT_RLX __ATOMIC_RELAXED, __HIP_MEMORY_SCOPE_AGENT
__device__ __forceinline__ void signal_done(unsigned* c, int tid) {
    asm volatile("s_waitcnt vmcnt(0)" ::: "memory");
    __syncthreads();
    if (tid == 0) __hip_atomic_fetch_add(c, 1u, AGENT_RLX);
}
__device__ __forceinline__ void wait_count(unsigned* c, unsigned want, int tid) {
    if (tid == 0) {
        unsigned sp = 0;
        while (__hip_atomic_load(c, AGENT_RLX) < want && ++sp < (1u << 20)) __builtin_amdgcn_s_sleep(4);
    }
    __syncthreads();
}
__device__ __forceinline__ void wait_counts(unsigned* c, int n, unsigned want, int tid) {
    if (tid < 64) {
        for (unsigned sp = 0; sp < (1u << 20); ++sp) {
            const unsigned v = tid < n ? __hip_atomic_load(c + 16 * tid, AGENT_RLX) : want;
            if (!__any(v < want)) break;
            __builtin_amdgcn_s_sleep(4);
        }
    }
    __syncthreads();
}
__device__ __forceinline__ bool has_sent(u32x4 v) { return (v[0] == SENT) | (v[1] == SENT) | (v[2] == SENT) | (v[3] == SENT); }

template <class RS>
__device__ __forceinline__ void task_f1(const Params& p, RS rs, RS rw, unsigned parity, LAS unsigned char* L, int sig, int hg, int wave) {
    const int lane = lane_id();
    const int tid = wave * 64 + lane;
    const int rho = lane & 15, q = lane >> 4;
    const int h = hg * 4 + wave;
    unsigned* ctrl = (unsigned*)(p.ws + WS_CTRL);
    const unsigned lds0 = (unsigned)(uintptr_t)L;
#pragma unroll
    for (int i = 0; i < 14; ++i) glds16(p.ws + WS_XF + ((size_t)sig * 56 + wave * 14 + i) * 1024 + lane * 16, (unsigned)__builtin_amdgcn_readfirstlane(lds0 + (wave * 14 + i) * 1024));
    wait_counts(gcnt(p, parity) + 16 * (2 * sig), 2, 56, tid);
    f32x4 sx[4][2], sm[4];
#pragma unroll
    for (int i = 0; i < 4; ++i) { sx[i][0] = f32x4{0, 0, 0, 0}; sx[i][1] = f32x4{0, 0, 0, 0}; sm[i] = f32x4{0, 0, 0, 0}; }
    u32x4 gf[2][28];
#pragma unroll
    for (int sg = 0; sg < 2; ++sg)
#pragma unroll
        for (int f = 0; f < 28; ++f) gf[sg][f] = __builtin_amdgcn_raw_buffer_load_b128(rs, R_GF + h * 65536 + gmap((f / KK) * 16384 + (f % KK) * 1024) + lane * 16, seg_base(2 * sig + sg, parity), 16);
    asm volatile("s_waitcnt vmcnt(56)\n\ts_barrier" ::: "memory");
    const LAS unsigned char* xb = L + lane * 16;
#pragma unroll
    for (int sg = 0; sg < 2; ++sg)
#pragma unroll
        for (int kk = 0; kk < KK; ++kk)
#pragma unroll
            for (int it = 0; it < 2; ++it) {
                u32x4 xf = *(const LAS u32x4*)(xb + (sg * 28 + it * KK + kk) * 1024);
                sx[sg * 2 + it][0] = mfma32(gf[sg][kk], xf, sx[sg * 2 + it][0]);
                sx[sg * 2 + it][1] = mfma32(gf[sg][KK + kk], xf, sx[sg * 2 + it][1]);
            }
    wait_count(ctrl + CT_M + 16 * sig, 28, tid);
    u32x4 mg[KK];
#pragma unroll
    for (int kk = 0; kk < KK; ++kk) mg[kk] = __builtin_amdgcn_raw_buffer_load_b128(rw, (unsigned)WS_MG + (unsigned)((sig * 16 + h) * KK + kk) * 1024 + lane * 16, 0, 16);
#pragma unroll
    for (int kk = 0; kk < KK; ++kk)
#pragma unroll
        for (int tt = 0; tt < 4; ++tt) sm[tt] = mfma32(mg[kk], *(const LAS u32x4*)(xb + (tt * KK + kk) * 1024), sm[tt]);
    const float NEG = -1.0e9f * LOG2E;
#pragma unroll
    for (int tt = 0; tt < 4; ++tt) {
        const bool mvalid = (q >> 1) == (tt >> 1);
        const int qi = perm32(rho, tt & 1);
        float v[8], m4[4];
#pragma unroll
        for (int kt = 0; kt < 2; ++kt)
#pragma unroll
            for (int r = 0; r < 4; ++r) { int key = 8 * q + 4 * kt + r; v[4 * kt + r] = key <= qi ? sx[tt][kt][r] : NEG; }
        float mx = fmaxf(fmaxf(fmaxf(v[0], v[1]), fmaxf(v[2], v[3])), fmaxf(fmaxf(v[4], v[5]), fmaxf(v[6], v[7])));
#pragma unroll
        for (int r = 0; r < 4; ++r) { m4[r] = sm[tt][r]; if (mvalid) mx = fmaxf(mx, m4[r]); }
        mx = xq_max(mx);
        float sum = 0.f;
#pragma unroll
        for (int r = 0; r < 8; ++r) { v[r] = __builtin_amdgcn_exp2f(v[r] - mx); sum += v[r]; }
#pragma unroll
        for (int r = 0; r < 4; ++r) { m4[r] = mvalid ? __builtin_amdgcn_exp2f(m4[r] - mx) : 0.f; sum += m4[r]; }
        const float inv = __builtin_amdgcn_rcpf(xq_sum(sum));
        u32x4 px = {pk2(v[0] * inv, v[1] * inv), pk2(v[2] * inv, v[3] * inv), pk2(v[4] * inv, v[5] * inv), pk2(v[6] * inv, v[7] * inv)};
        u32x2 pm = {pk2(m4[0] * inv, m4[1] * inv), pk2(m4[2] * inv, m4[3] * inv)};
        const unsigned fi = (unsigned)(((2 * sig + (tt >> 1)) * 16 + h) * 2 + (tt & 1));
        __builtin_amdgcn_raw_buffer_store_b128(px, rw, (unsigned)WS_PX + fi * 1024 + lane * 16, 0, 16);
        __builtin_amdgcn_raw_buffer_store_b64(pm, rw, mvalid ? (unsigned)WS_PM + fi * 256 + (lane & 31) * 8 : 0x3ffffff0u, 0, 16);
    }
    signal_done(ctrl + CT_SIG + 16 * sig, tid);
}

template <class RS>
__device__ __forceinline__ void task_f1f(const Params& p, RS rs, RS rw, unsigned parity, int sig, int h, int wave) {
    const int lane = lane_id();
    const int tid = wave * 64 + lane;
    const int rho = lane & 15, q = lane >> 4;
    unsigned* ctrl = (unsigned*)(p.ws + WS_CTRL);
    u32x4 xf[KK], gf[28];
#pragma unroll
    for (int kk = 0; kk < KK; ++kk) xf[kk] = *(const u32x4*)(p.ws + WS_XF + ((size_t)sig * 56 + wave * 14 + kk) * 1024 + lane * 16);
    wait_counts(gcnt(p, parity) + 16 * (2 * sig), 2, 56, tid);
#pragma unroll
    for (int f = 0; f < 28; ++f) gf[f] = __builtin_amdgcn_raw_buffer_load_b128(rs, R_GF + h * 65536 + gmap((f / KK) * 16384 + (f % KK) * 1024) + lane * 16, seg_base(2 * sig + (wave >> 1), parity), 16);
    f32x4 s0 = {0, 0, 0, 0}, s1 = s0, sm = s0;
#pragma unroll
    for (int kk = 0; kk < KK; ++kk) { s0 = mfma32(gf[kk], xf[kk], s0); s1 = mfma32(gf[KK + kk], xf[kk], s1); }
    const float NEG = -1.0e9f * LOG2E;
    const bool mvalid = (q >> 1) == (wave >> 1);
    const int qi = perm32(rho, wave & 1);
    float v[8], m4[4];
#pragma unroll
    for (int r = 0; r < 4; ++r) { v[r] = 8 * q + r <= qi ? s0[r] : NEG; v[4 + r] = 8 * q + 4 + r <= qi ? s1[r] : NEG; }
    float mx = fmaxf(fmaxf(fmaxf(v[0], v[1]), fmaxf(v[2], v[3])), fmaxf(fmaxf(v[4], v[5]), fmaxf(v[6], v[7])));
    if (tid == 0) {
        unsigned sp = 0;
        while (__hip_atomic_load(ctrl + CT_M + 16 * 30, AGENT_RLX) < 28u && ++sp < (1u << 22)) __builtin_amdgcn_s_sleep(1);
    }
    __syncthreads();
    u32x4 mg[KK];
    for (unsigned sp = 0; sp < (1u << 20); ++sp) {
        bool bad = false;
#pragma unroll
        for (int kk = 0; kk < KK; ++kk) mg[kk] = __builtin_amdgcn_raw_buffer_load_b128(rw, (unsigned)WS_MG + (unsigned)((sig * 16 + h) * KK + kk) * 1024 + lane * 16, 0, 16);
#pragma unroll
        for (int kk = 0; kk < KK; ++kk) bad |= has_sent(mg[kk]);
        if (sig < 31 || !__any(bad)) break;
        asm volatile("" ::: "memory");
    }
#pragma unroll
    for (int kk = 0; kk < KK; ++kk) sm = mfma32(mg[kk], xf[kk], sm);
#pragma unroll
    for (int r = 0; r < 4; ++r) { m4[r] = sm[r]; if (mvalid) mx = fmaxf(mx, m4[r]); }
    mx = xq_max(mx);
    float sum = 0.f;
#pragma unroll
    for (int r = 0; r < 8; ++r) { v[r] = __builtin_amdgcn_exp2f(v[r] - mx); sum += v[r]; }
#pragma unroll
    for (int r = 0; r < 4; ++r) { m4[r] = mvalid ? __builtin_amdgcn_exp2f(m4[r] - mx) : 0.f; sum += m4[r]; }
    const float inv = __builtin_amdgcn_rcpf(xq_sum(sum));
    u32x4 px = {pk2(v[0] * inv, v[1] * inv), pk2(v[2] * inv, v[3] * inv), pk2(v[4] * inv, v[5] * inv), pk2(v[6] * inv, v[7] * inv)};
    u32x2 pm = {pk2(m4[0] * inv, m4[1] * inv), pk2(m4[2] * inv, m4[3] * inv)};
    const unsigned fi = (unsigned)(((2 * sig + (wave >> 1)) * 16 + h) * 2 + (wave & 1));
    __builtin_amdgcn_raw_buffer_store_b128(px, rw, (unsigned)WS_PX + fi * 1024 + lane * 16, 0, 16);
    __builtin_amdgcn_raw_buffer_store_b64(pm, rw, mvalid ? (unsigned)WS_PM + fi * 256 + (lane & 31) * 8 : 0x3ffffff0u, 0, 16);
    signal_done(ctrl + CT_SIG + 16 * sig, tid);
}

template <class RS>
__device__ __forceinline__ void task_t2(const Params& p, RS rs, RS rw, unsigned parity, LAS unsigned char* L, int seg, int qd, int wave) {
    const int lane = lane_id();
    const int tid = wave * 64 + lane; (void)tid;
    const int rho = lane & 15, q = lane >> 4;
    const int cp = 4 * qd + wave < 14 ? 4 * qd + wave : 13;
    const int ct0 = 2 * cp, ct1 = ct0 + 1;
    u32x4 uf[2][16];
    wait_count(gcnt(p, parity) + 16 * seg, 56, tid);
#pragma unroll
    for (int h = 0; h < 16; ++h) {
        uf[0][h] = __builtin_amdgcn_raw_buffer_load_b128(rs, R_UF + ct0 * 32768 + gmap(h * 1024) + lane * 16, seg_base(seg, parity), 16);
        uf[1][h] = __builtin_amdgcn_raw_buffer_load_b128(rs, R_UF + ct1 * 32768 + gmap(h * 1024) + lane * 16, seg_base(seg, parity), 16);
    }
    if (tid == 0) {
        unsigned* c1 = (unsigned*)(p.ws + WS_CTRL) + CT_SIG + 16 * (seg >> 1);
        unsigned* c2 = (unsigned*)(p.ws + WS_CTRL) + CT_M + 16 * ((seg >> 1) + 1);
        const unsigned w2 = seg >= 62 ? 112u : 1u;
        unsigned sp = 0;
        const unsigned w1 = seg >= 62 ? 0u : seg >= 60 ? 16u : 4u;
        while ((__hip_atomic_load(c1, AGENT_RLX) < w1 || __hip_atomic_load(c2, AGENT_RLX) < w2) && ++sp < (1u << 22)) __builtin_amdgcn_s_sleep(1);
    }
    __syncthreads();
    u32x4 wf[2][8], st[10];
#pragma unroll
    for (int hp = 0; hp < 8; ++hp) {
        wf[0][hp] = __builtin_amdgcn_raw_buffer_load_b128(rw, (unsigned)WS_MW + (unsigned)((seg * 28 + ct0) * 8 + hp) * 1024 + lane * 16, 0, 16);
        wf[1][hp] = __builtin_amdgcn_raw_buffer_load_b128(rw, (unsigned)WS_MW + (unsigned)((seg * 28 + ct1) * 8 + hp) * 1024 + lane * 16, 0, 16);
    }
    for (unsigned sp = 0; sp < (1u << 20); ++sp) {
        bool bad = false;
#pragma unroll
        for (int i = 0; i < 8; ++i) st[i] = __builtin_amdgcn_raw_buffer_load_b128(rw, (unsigned)WS_PX + seg * 32768 + i * 4096 + tid * 16, 0, 16);
#pragma unroll
        for (int i = 0; i < 2; ++i) st[8 + i] = __builtin_amdgcn_raw_buffer_load_b128(rw, (unsigned)WS_PM + seg * 8192 + i * 4096 + tid * 16, 0, 16);
        if (seg < 62) break;
#pragma unroll
        for (int i = 0; i < 10; ++i) bad |= has_sent(st[i]);
        if (!__any(bad)) break;
        asm volatile("" ::: "memory");
    }
#pragma unroll
    for (int i = 0; i < 10; ++i) *(LAS u32x4*)(L + i * 4096 + tid * 16) = st[i];
    __syncthreads();
    f32x4 acc[2][2], accm[2][2];
#pragma unroll
    for (int a = 0; a < 2; ++a)
#pragma unroll
        for (int t = 0; t < 2; ++t) { acc[a][t] = f32x4{0, 0, 0, 0}; accm[a][t] = f32x4{0, 0, 0, 0}; }
#pragma unroll
    for (int h = 0; h < 16; ++h)
#pragma unroll
        for (int t = 0; t < 2; ++t) {
            u32x4 px = *(const LAS u32x4*)(L + (h * 2 + t) * 1024 + lane * 16);
#pragma unroll
            for (int a = 0; a < 2; ++a) acc[a][t] = mfma32(uf[a][h], px, acc[a][t]);
            if (h & 1) {
                u32x2 pa = *(const LAS u32x2*)(L + 32768 + ((h - 1) * 2 + t) * 256 + (lane & 31) * 8), pb = *(const LAS u32x2*)(L + 32768 + (h * 2 + t) * 256 + (lane & 31) * 8);
                u32x4 pm = {pa[0], pa[1], pb[0], pb[1]};
#pragma unroll
                for (int a = 0; a < 2; ++a) accm[a][t] = mfma32(wf[a][h >> 1], pm, accm[a][t]);
            }
        }
#pragma unroll
    for (int a = 0; a < 2; ++a)
#pragma unroll
        for (int t = 0; t < 2; ++t) {
            f32x4 o = acc[a][t] + accm[a][t];
            const int tok = seg * 32 + perm32(rho, t), ct = a ? ct1 : ct0;
            *(u32x2*)(p.out + (size_t)tok * 896 + 16 * ct + 4 * q) = u32x2{pk2(o[0], o[1]), pk2(o[2], o[3])};
        }
    __syncthreads();
}

template <class RS>
__device__ __forceinline__ void task_kv(const Params& p, RS rw, LAS unsigned char* L, int h, int wave) {
    const int lane = lane_id();
    const int tid = wave * 64 + lane; (void)tid;
    const int rho = lane & 15, q = lane >> 4;
    LAS float* kvs = (LAS float*)L;
    wait_count((unsigned*)(p.ws + WS_CTRL) + CT_P1, 64, tid);
#pragma unroll
    for (int z = 0; z < 2; ++z) {
        const int fd = wave * 2 + z;
        f32x4 v[4];
#pragma unroll
        for (int c = 0; c < 4; ++c) v[c] = __builtin_bit_cast(f32x4, __builtin_amdgcn_raw_buffer_load_b128(rw, (unsigned)WS_KVP + (unsigned)((c * 16 + h) * 512 + fd * 64 + lane) * 16, 0, 16));
        f32x4 s = v[0];
#pragma unroll
        for (int c = 1; c < 4; ++c) s += v[c];
#pragma unroll
        for (int r = 0; r < 4; ++r) kvs[(16 * (fd >> 1) + 4 * q + r) * 33 + 16 * (fd & 1) + rho] = s[r];
    }
    __syncthreads();
    const int pn = wave >> 1, dt = wave & 1, d = perm32(rho, dt);
    float v[8];
#pragma unroll
    for (int j = 0; j < 8; ++j) v[j] = kvs[(32 * pn + 8 * q + j) * 33 + d];
    u32x4 o = {pk2(v[0], v[1]), pk2(v[2], v[3]), pk2(v[4], v[5]), pk2(v[6], v[7])};
    __builtin_amdgcn_raw_buffer_store_b128(o, rw, (unsigned)WS_KVF + (h * 4 + wave) * 1024 + lane * 16, 0, 16);
    signal_done((unsigned*)(p.ws + WS_CTRL) + CT_KV, tid);
}

template <class RS>
__device__ __forceinline__ void task_pb2(const Params& p, RS rw, LAS unsigned char* L, int task, int wave) {
    const int lane = lane_id();
    const int tid = wave * 64 + lane; (void)tid;
    const int rho = lane & 15, q = lane >> 4;
    wait_count((unsigned*)(p.ws + WS_CTRL) + CT_KV, 16, tid);
#pragma unroll
    for (int hh = 0; hh < 4; ++hh) {
        const int h = wave * 4 + hh;
        u32x4 kf[4], fp[4], fn[4];
#pragma unroll
        for (int i = 0; i < 4; ++i) kf[i] = __builtin_amdgcn_raw_buffer_load_b128(rw, (unsigned)WS_KVF + (h * 4 + i) * 1024 + lane * 16, 0, 16);
#pragma unroll
        for (int tt = 0; tt < 4; ++tt) {
            const unsigned src = (unsigned)WS_PHQ + (unsigned)(((task * 4 + tt) * 16 + h) * 2) * 1024 + lane * 16;
            fp[tt] = __builtin_amdgcn_raw_buffer_load_b128(rw, src, 0, 16); fn[tt] = __builtin_amdgcn_raw_buffer_load_b128(rw, src + 1024, 0, 16);
        }
#pragma unroll
        for (int tt = 0; tt < 4; ++tt) {
            f32x4 n0 = mfma32(kf[0], fp[tt], f32x4{0, 0, 0, 0}); n0 = mfma32(kf[2], fn[tt], n0);
            f32x4 n1 = mfma32(kf[1], fp[tt], f32x4{0, 0, 0, 0}); n1 = mfma32(kf[3], fn[tt], n1);
            float den = __shfl(n1[0], 48 + rho) + 1e-6f;
            float inv = __builtin_amdgcn_rcpf(den);
            if (q == 3) n1 = f32x4{0, 0, 0, 0};
            *(LAS u32x4*)(L + ((tt * 16 + h) * 64 + lane) * 16) = pk8(n0 * inv, n1 * inv);
        }
    }
    u32x4 wf[2][16];
#pragma unroll
    for (int z = 0; z < 2; ++z)
#pragma unroll
        for (int h = 0; h < 16; ++h) wf[z][h] = __builtin_amdgcn_raw_buffer_load_b128(rw, (unsigned)WS_BWO + (unsigned)((wave * 7 + z) * 16 + h) * 1024 + lane * 16, 0, 16);
    __syncthreads();
#pragma unroll
    for (int ci = 0; ci < 7; ++ci) {
        const int ct = wave * 7 + ci;
        f32x4 acc[4];
#pragma unroll
        for (int tt = 0; tt < 4; ++tt) acc[tt] = f32x4{0, 0, 0, 0};
#pragma unroll
        for (int h = 0; h < 16; ++h) {
#pragma unroll
            for (int tt = 0; tt < 4; ++tt) acc[tt] = mfma32(wf[ci & 1][h], *(const LAS u32x4*)(L + ((tt * 16 + h) * 64 + lane) * 16), acc[tt]);
            if (ci + 2 < 7) wf[ci & 1][h] = __builtin_amdgcn_raw_buffer_load_b128(rw, (unsigned)WS_BWO + (unsigned)((ct + 2) * 16 + h) * 1024 + lane * 16, 0, 16);
        }
#pragma unroll
        for (int tt = 0; tt < 4; ++tt) {
            int tok = task * 64 + (tt >> 1) * 32 + perm32(rho, tt & 1);
            *(u32x2*)(p.out + (size_t)tok * 896 + 448 + 16 * ct + 4 * q) = u32x2{pk2(acc[tt][0], acc[tt][1]), pk2(acc[tt][2], acc[tt][3])};
        }
    }
    __syncthreads();
}

__device__ __forceinline__ void phi_pair(f32x4 a0, f32x4 a1, bool tail, u32x4& op, u32x4& on) {
    float v[8];
#pragma unroll
    for (int r = 0; r < 4; ++r) { v[r] = a0[r]; v[4 + r] = tail ? a0[r] : a1[r]; }
    float mx = fmaxf(fmaxf(fmaxf(v[0], v[1]), fmaxf(v[2], v[3])), fmaxf(fmaxf(v[4], v[5]), fmaxf(v[6], v[7])));
    float mn = fminf(fminf(fminf(v[0], v[1]), fminf(v[2], v[3])), fminf(fminf(v[4], v[5]), fminf(v[6], v[7])));
    mx = xq_max(mx); mn = -xq_max(-mn);
    float ep[8], en[8], sp = 0.f, sn = 0.f;
#pragma unroll
    for (int r = 0; r < 8; ++r) {
        ep[r] = __builtin_amdgcn_exp2f(v[r] - mx); en[r] = __builtin_amdgcn_exp2f(mn - v[r]);
        if (r >= 4 && tail) { ep[r] = 0.f; en[r] = 0.f; }
        sp += ep[r]; sn += en[r];
    }
    sp = __builtin_amdgcn_rcpf(xq_sum(sp)); sn = __builtin_amdgcn_rcpf(xq_sum(sn));
    op = u32x4{pk2(ep[0] * sp, ep[1] * sp), pk2(ep[2] * sp, ep[3] * sp), pk2(ep[4] * sp, ep[5] * sp), pk2(ep[6] * sp, ep[7] * sp)};
    on = u32x4{pk2(en[0] * sn, en[1] * sn), pk2(en[2] * sn, en[3] * sn), pk2(en[4] * sn, en[5] * sn), pk2(en[6] * sn, en[7] * sn)};
}
template <class RS>
__device__ __forceinline__ void task_p1(const Params& p, RS rw, LAS unsigned char* L, int task, int wave) {
    const int lane = lane_id();
    const int tid = wave * 64 + lane;
    const int rho = lane & 15, q = lane >> 4;
    const int chunk = task >> 4, h = task & 15;
    const unsigned lds0 = (unsigned)(uintptr_t)L;
#pragma unroll
    for (int w3 = 0; w3 < 3; ++w3)
#pragma unroll
        for (int i = 0; i < 7; ++i)
            glds16(p.ws + WS_BW + (size_t)(w3 * 16 + h) * 28672 + (wave * 7 + i) * 1024 + lane * 16, (unsigned)__builtin_amdgcn_readfirstlane(lds0 + w3 * 28672 + (wave * 7 + i) * 1024));
    f32x4 kvacc[8];
#pragma unroll
    for (int i = 0; i < 8; ++i) kvacc[i] = f32x4{0, 0, 0, 0};
    u32x4 xn[28];
    const unsigned char* xsrc = p.ws + WS_BXF + ((size_t)(chunk * 16 + wave) * 28) * 1024 + lane * 16;
#pragma unroll
    for (int f = 0; f < 28; ++f) xn[f] = *(const u32x4*)(xsrc + f * 1024);
    asm volatile("s_waitcnt vmcnt(28)\n\ts_barrier" ::: "memory");
    LAS unsigned char* tr = L + 86016 + wave * 4608;
    for (int rd = 0; rd < 4; ++rd) {
        asm volatile("" ::: "memory");
        const int tok0 = chunk * 512 + rd * 128 + wave * 32;
        const unsigned char* xnext = xsrc + (size_t)(rd < 3 ? rd + 1 : rd) * 4 * 28 * 1024;
        f32x4 aq[2][2], ak[2][2], av[2][2];
#pragma unroll
        for (int a = 0; a < 2; ++a)
#pragma unroll
            for (int c = 0; c < 2; ++c) { aq[a][c] = f32x4{0, 0, 0, 0}; ak[a][c] = f32x4{0, 0, 0, 0}; av[a][c] = f32x4{0, 0, 0, 0}; }
        const LAS unsigned char* wb = L + lane * 16;
#pragma unroll
        for (int kk = 0; kk < KK; ++kk) {
#pragma unroll
            for (int a = 0; a < 2; ++a) {
                u32x4 wqf = *(const LAS u32x4*)(wb + (a * KK + kk) * 1024), wkf = *(const LAS u32x4*)(wb + 28672 + (a * KK + kk) * 1024), wvf = *(const LAS u32x4*)(wb + 57344 + (a * KK + kk) * 1024);
#pragma unroll
                for (int tt = 0; tt < 2; ++tt) {
                    aq[a][tt] = mfma32(wqf, xn[tt * KK + kk], aq[a][tt]);
                    ak[a][tt] = mfma32(wkf, xn[tt * KK + kk], ak[a][tt]);
                    av[a][tt] = mfma32(xn[tt * KK + kk], wvf, av[a][tt]);
                }
            }
            xn[kk] = *(const u32x4*)(xnext + kk * 1024); xn[KK + kk] = *(const u32x4*)(xnext + (KK + kk) * 1024);
        }
        const bool tail = q == 3;
#pragma unroll
        for (int tt = 0; tt < 2; ++tt) {
            u32x4 op, on;
            phi_pair(ak[0][tt], ak[1][tt], tail, op, on);
            LAS unsigned char* d = tr + perm32(rho, tt) * 144 + q * 16;
            *(LAS u32x4*)d = op; *(LAS u32x4*)(d + 64) = on;
        }
#pragma unroll
        for (int tt = 0; tt < 2; ++tt) {
            u32x4 op, on;
            phi_pair(aq[0][tt], aq[1][tt], tail, op, on);
            const unsigned dst = (unsigned)WS_PHQ + (unsigned)((((tok0 >> 4) + tt) * 16 + h) * 2) * 1024 + lane * 16;
            __builtin_amdgcn_raw_buffer_store_b128(op, rw, dst, 0, 16); __builtin_amdgcn_raw_buffer_store_b128(on, rw, dst + 1024, 0, 16);
        }
        u32x4 fv[2] = {pk8(av[0][0], av[0][1]), pk8(av[1][0], av[1][1])};
        if (rho == 12) fv[1] = u32x4{0x3F803F80u, 0x3F803F80u, 0x3F803F80u, 0x3F803F80u};
        asm volatile("s_waitcnt lgkmcnt(0)" ::: "memory");
#pragma unroll
        for (int ft = 0; ft < 4; ++ft) {
            const LAS unsigned char* base = tr + (8 * q + (rho >> 2)) * 144 + (16 * ft + 4 * (rho & 3)) * 2;
            u32x2 l2 = __builtin_bit_cast(u32x2, __builtin_amdgcn_ds_read_tr16_b64_v4i16((LAS s4v*)base));
            u32x2 h2 = __builtin_bit_cast(u32x2, __builtin_amdgcn_ds_read_tr16_b64_v4i16((LAS s4v*)(base + 4 * 144)));
            u32x4 fk = {l2[0], l2[1], h2[0], h2[1]};
#pragma unroll
            for (int dt = 0; dt < 2; ++dt) kvacc[ft * 2 + dt] = mfma32(fk, fv[dt], kvacc[ft * 2 + dt]);
        }
    }
    __syncthreads();
    LAS f32x4* red = (LAS f32x4*)L;
#pragma unroll
    for (int i = 0; i < 8; ++i) red[(wave * 8 + i) * 64 + lane] = kvacc[i];
    __syncthreads();
#pragma unroll
    for (int z = 0; z < 2; ++z) {
        const int i = tid + 256 * z;
        f32x4 s = (red[i] + red[512 + i]) + (red[1024 + i] + red[1536 + i]);
        __builtin_amdgcn_raw_buffer_store_b128(__builtin_bit_cast(u32x4, s), rw, (unsigned)WS_KVP + (unsigned)((chunk * 16 + h) * 512 + i) * 16, 0, 16);
    }
    signal_done((unsigned*)(p.ws + WS_CTRL) + CT_P1, tid);
}

#define MFA(d, a, b) asm volatile("v_mfma_f32_16x16x32_bf16 %0, %1, %2, %0" : "+v"(d) : "a"(a), "v"(b))
#define MFB(d, a, b) asm volatile("v_mfma_f32_16x16x32_bf16 %0, %1, %2, %0" : "+v"(d) : "v"(a), "a"(b))
#define MF0(d, a, b) do { if (isG) asm volatile("v_mfma_f32_16x16x32_bf16 %0, %1, %2, 0" : "=&v"(d) : "a"(a), "v"(b)); else asm volatile("v_mfma_f32_16x16x32_bf16 %0, %1, %2, 0" : "=&v"(d) : "v"(b), "a"(a)); } while (0)
template <bool isG, class RS>
__device__ __forceinline__ void gu_gemm(const Params& p, RS rs, RS rw, unsigned parity, LAS unsigned char* L, int wg, int odd, int wave) {
    const int lane = lane_id();
    const int T0 = wg * 16 + wave * 4;
    const unsigned lds0 = (unsigned)(uintptr_t)L;
    const unsigned char* xsrc = p.ws + WS_XF + (size_t)odd * 28672 + wave * 7168 + lane * 16;
#pragma unroll
    for (int b = 0; b < 2; ++b)
#pragma unroll
        for (int i = 0; i < 7; ++i) glds16(xsrc + (size_t)b * 57344 + i * 1024, (unsigned)__builtin_amdgcn_readfirstlane(lds0 + b * 28672 + wave * 7168 + i * 1024));
    const unsigned char* wsrc = p.ws + (isG ? WS_WA : WS_WB) + (size_t)T0 * KK * 1024 + lane * 16;
    u32x4 w[4][KK];
#pragma unroll
    for (int kk = 0; kk < KK; ++kk) {
#pragma unroll
        for (int t = 0; t < 4; ++t) w[t][kk] = *(const u32x4*)(wsrc + (t * KK + kk) * 1024);
        if (kk == 10) asm volatile("s_waitcnt vmcnt(44)\n\ts_barrier" ::: "memory");
    }
    const int h = T0 / 28, t28 = T0 % 28;
    const unsigned obase = (isG ? R_GF + h * 65536 + gmap((t28 >> 1) * 1024) : R_UF + t28 * 32768 + gmap(h * 1024)) + lane * 16;
    const unsigned cbase = HO_CNT + (parity & 1u) * 8192u + odd * 64;
    const unsigned smask = __builtin_amdgcn_readfirstlane(wave == 0 ? 1u : 0u);
    const unsigned one = 1;
    u32x4 xf[28];
#pragma unroll
    for (int f = 0; f < 28; ++f) xf[f] = *(const LAS u32x4*)(L + lane * 16 + f * 1024);
    f32x4 accA[4][2], accB[4][2];
    auto put = [&](f32x4 (&a)[4][2], int k, unsigned so) {
        if (isG) __builtin_amdgcn_raw_buffer_store_b128(pk8(a[k & 2][k & 1], a[(k & 2) + 1][k & 1]), rs, obase + (k & 1) * 32768 + (k >> 1) * 1024, so, 16);
        else __builtin_amdgcn_raw_buffer_store_b128(pk8(a[k][0], a[k][1]), rs, obase + k * 32768, so, 16);
    };
    auto signal = [&](int it) {
        unsigned long long sv;
        asm volatile("s_mov_b64 %0, exec\n\ts_mov_b32 exec_lo, %5\n\ts_mov_b32 exec_hi, 0\n\tbuffer_atomic_add %1, %2, %3, %4 offen\n\ts_mov_b64 exec, %0"
                     : "=&s"(sv) : "v"(one), "v"(cbase), "s"(rs), "s"(it * 128), "s"(smask) : "memory");
    };
    auto body = [&](f32x4 (&acc)[4][2], f32x4 (&prev)[4][2], int it, auto mode) {
        constexpr int MODE = decltype(mode)::value;
        const LAS unsigned char* xb = L + ((it + 1) & 3) * 28672 + lane * 16;
        const int itn = it + 4 < 32 ? it + 4 : 31;
        const unsigned char* ssrc = xsrc + (size_t)itn * 57344;
        const unsigned sdst = lds0 + (it & 3) * 28672 + wave * 7168;
        const unsigned sop = seg_base(2 * (it - 1) + odd, parity);
#pragma unroll
        for (int kk = 0; kk < KK; ++kk) {
#pragma unroll
            for (int tt = 0; tt < 2; ++tt) {
#pragma unroll
                for (int t = 0; t < 4; ++t) {
                    if (kk == 0) MF0(acc[t][tt], w[t][0], xf[tt * KK]);
                    else if (isG) MFA(acc[t][tt], w[t][kk], xf[tt * KK + kk]); else MFB(acc[t][tt], xf[tt * KK + kk], w[t][kk]);
                }
                if (kk == 0 && tt == 0) {
                    if (MODE == 0) LDS_BAR(); else asm volatile("s_waitcnt vmcnt(5) lgkmcnt(0)\n\ts_barrier" ::: "memory");
                    if (MODE == 2 || MODE == 3) signal(it > 3 ? it - 2 : 40);
                }
                { const int g = kk * 2 + tt - 2; if (g >= 0) xf[(g & 1) * KK + (g >> 1)] = *(const LAS u32x4*)(xb + ((g & 1) * KK + (g >> 1)) * 1024); }
            }
            if ((MODE == 2 || MODE == 3) && kk >= 1 && kk <= 4) put(prev, kk - 1, sop);
            if (MODE != 0 && kk >= 5 && kk < 12) glds16(ssrc + (kk - 5) * 1024, (unsigned)__builtin_amdgcn_readfirstlane(sdst + (kk - 5) * 1024));
        }
        xf[KK - 1] = *(const LAS u32x4*)(xb + (KK - 1) * 1024);
        xf[2 * KK - 1] = *(const LAS u32x4*)(xb + (2 * KK - 1) * 1024);
        if (MODE < 2 || MODE == 3) {
            asm volatile("s_nop 7\n\ts_nop 1" : "+v"(acc[0][0]), "+v"(acc[0][1]), "+v"(acc[1][0]), "+v"(acc[1][1]), "+v"(acc[2][0]), "+v"(acc[2][1]), "+v"(acc[3][0]), "+v"(acc[3][1]));
#pragma unroll
            for (int k = 0; k < 4; ++k) put(acc, k, seg_base(2 * it + odd, parity));
            asm volatile("s_waitcnt vmcnt(0) lgkmcnt(0)\n\ts_barrier" ::: "memory");
            signal(it);
            if (MODE == 3) signal(it - 1);
            if (MODE == 0) {
#pragma unroll
                for (int b = 2; b < 5; ++b)
#pragma unroll
                    for (int i = 0; i < 7; ++i) glds16(xsrc + (size_t)b * 57344 + i * 1024, (unsigned)__builtin_amdgcn_readfirstlane(lds0 + (b & 3) * 28672 + wave * 7168 + i * 1024));
            }
        }
    };
    body(accA, accB, 0, std::integral_constant<int, 0>{});
    body(accB, accA, 1, std::integral_constant<int, 1>{});
    body(accA, accB, 2, std::integral_constant<int, 4>{});
    for (int it = 3; it < 31; it += 2) { body(accB, accA, it, std::integral_constant<int, 2>{}); body(accA, accB, it + 1, std::integral_constant<int, 2>{}); }
    body(accB, accA, 31, std::integral_constant<int, 3>{});
}

template <bool isG, class RS>
__device__ __forceinline__ void task_mp(const Params& p, RS rs, RS rw, unsigned parity, LAS unsigned char* L, int wg, int sig0, int sig1, int wave) {
    const bool prompt = sig1 == 32;
    const int lane = lane_id();
    const int rho = lane & 15, q = lane >> 4;
    const int T0 = wg * 16 + wave * 4;
    const unsigned char* wsrc = p.ws + (isG ? WS_WA : WS_WB) + (size_t)T0 * KK * 1024 + lane * 16;
    u32x4 w[4][KK];
#pragma unroll
    for (int t = 0; t < 4; ++t)
#pragma unroll
        for (int kk = 0; kk < KK; ++kk) w[t][kk] = *(const u32x4*)(wsrc + (t * KK + kk) * 1024);
    const int h = T0 / 28, t28 = T0 % 28;
    const unsigned mbase = (unsigned)WS_MG + (unsigned)(h * KK + (t28 >> 1)) * 1024 + lane * 16;
    unsigned* ctrl = (unsigned*)(p.ws + WS_CTRL);
    const int i8 = rho & 7, par = rho >> 3;
    const unsigned mlane = (unsigned)((q >> 1) * 256 + (i8 >> 1) * 64 + (q & 1) * 32 + (i8 & 1) * 16);
    auto mload = [&](int sig, u32x4 (&v)[4]) {
        const int s0 = 2 * sig, s1 = s0 + 1;
        const unsigned b0 = HO_WC + s0 * 16384 + (parity & 1u) * 8192u, b1 = b0 + 16384; (void)s1;
        const unsigned base = (par ? b1 : b0) + mlane;
#pragma unroll
        for (int k = 0; k < 4; ++k) { int kk = wave + 4 * k; kk = kk < KK ? kk : wave; v[k] = __builtin_amdgcn_raw_buffer_load_b128(rs, base + kk * 512, 0, 16); }
    };
    u32x4 v[4];
    mload(sig0, v);
    for (int sig = sig0; sig < sig1; ++sig) {
        LAS unsigned char* mimg = L + (sig & 1) * 16384;
        for (unsigned sp = 0; sp < (1u << 18); ++sp) {
            bool bad = false;
#pragma unroll
            for (int k = 0; k < 4; ++k) bad |= has_sent(v[k]);
            if (sig == 0 && par == 0) bad = false;
            if (!__any(bad)) break;
            if (sig < 31) __builtin_amdgcn_s_sleep(2);
            asm volatile("" ::: "memory");
            mload(sig, v);
        }
#pragma unroll
        for (int k = 0; k < 4; ++k) { int kk = wave + 4 * k; kk = kk < KK ? kk : wave; *(LAS u32x4*)(mimg + kk * 1024 + lane * 16) = v[k]; }
        asm volatile("s_waitcnt vmcnt(2) lgkmcnt(0)\n\ts_barrier" ::: "memory");
        if (!prompt && sig > sig0 + 1 && wave == 0 && lane == 0) __hip_atomic_fetch_add(ctrl + CT_M + 16 * (sig - 2), 1u, AGENT_RLX);
        mload(sig + 1 < sig1 ? sig + 1 : sig, v);
        f32x4 a[4];
        u32x4 mf[KK];
#pragma unroll
        for (int kk = 0; kk < KK; ++kk) mf[kk] = *(const LAS u32x4*)(mimg + kk * 1024 + lane * 16);
#pragma unroll
        for (int kk = 0; kk < KK; ++kk)
#pragma unroll
            for (int t = 0; t < 4; ++t) { if (kk == 0) MF0(a[t], w[t][0], mf[0]); else MFA(a[t], w[t][kk], mf[kk]); }
        asm volatile("s_nop 7\n\ts_nop 1" : "+v"(a[0]), "+v"(a[1]), "+v"(a[2]), "+v"(a[3]));
        const unsigned so = (unsigned)sig * (224u * 1024u);
        __builtin_amdgcn_raw_buffer_store_b128(pk8(a[0], a[1]), rw, mbase, so, 16);
        __builtin_amdgcn_raw_buffer_store_b128(pk8(a[2], a[3]), rw, mbase + 1024, so, 16);
        if (prompt) signal_done(ctrl + CT_M + 16 * sig, wave * 64 + lane);
    }
    asm volatile("s_waitcnt vmcnt(0)" ::: "memory");
    __syncthreads();
    if (!prompt && wave == 0 && lane < 2 && sig1 - 1 - lane >= sig0) __hip_atomic_fetch_add(ctrl + CT_M + 16 * (sig1 - 1 - lane), 1u, AGENT_RLX);
}

template <class RS>
__device__ __forceinline__ void worker_loop(const Params& p, RS rs, RS rw, unsigned parity, LAS unsigned char* L, int x, int wt, int wave) {
    unsigned* ctrl = (unsigned*)(p.ws + WS_CTRL);
    LAS unsigned* word = (LAS unsigned*)(L + 130048);
    if (wt < 28) {
        const int g = wt % 14, odd = wt / 14;
        if (g < 7) gu_gemm<true>(p, rs, rw, parity, L, x * 7 + g, odd, wave);
        else gu_gemm<false>(p, rs, rw, parity, L, x * 7 + g - 7, odd, wave);
    }
    for (;;) {
        if (wave == 0 && lane_id() == 0) *word = __hip_atomic_fetch_add(ctrl + CT_Q + 16 * x, 1u, AGENT_RLX);
        __syncthreads();
        int t = (int)__builtin_amdgcn_readfirstlane(*word);
        __syncthreads();
        if (t >= 16 + 4 * 7 + 38 + 64 + 12) { break; }
        if (t < 16) { task_p1(p, rw, L, t * 4 + x, wave); continue; }
        t -= 16;
        int sig0 = 0, n = 8, pn = 0, kind = -1, arg = 0;
        for (int b = 0; b < 9; ++b) {
            n = b < 2 ? 8 : b == 2 ? 6 : b == 3 ? 4 : b < 6 ? 2 : b < 8 ? 1 : 0;
            const int nmp = b < 4 ? 7 : 0, ex = b == 1 ? 4 : b == 2 ? 8 : 0;
            if (t < nmp) { kind = 0; arg = t; break; } t -= nmp;
            if (t < ex) { kind = b == 1 ? 3 : 4; arg = t; break; } t -= ex;
            const int nf = b >= 6 ? 4 * n : n;
            if (t < nf) { kind = b >= 6 ? 5 : 1; arg = t; break; } t -= nf;
            if (t < 2 * pn) { kind = 2; arg = t; break; } t -= 2 * pn;
            sig0 += n; pn = n;
        }
        if (kind == 5) { task_f1f(p, rs, rw, parity, sig0, x * 4 + arg, wave); continue; }
        if (kind == 0) {
            task_mp<true>(p, rs, rw, parity, L, x * 7 + arg, sig0, sig0 == 22 ? 32 : sig0 + n, wave);
        }
        else if (kind == 1) task_f1(p, rs, rw, parity, L, sig0 + arg, x, wave);
        else if (kind == 2) task_t2(p, rs, rw, parity, L, 2 * (sig0 - pn) + arg, x, wave);
        else if (kind == 3) task_kv(p, rw, L, x + 4 * arg, wave);
        else task_pb2(p, rw, L, x * 8 + arg, wave);
    }
}

__global__ void __launch_bounds__(256, 1) chain_kernel(Params p) {
    extern __shared__ __attribute__((aligned(16))) unsigned char smem[];
    LAS unsigned char* L = (LAS unsigned char*)smem;
    constexpr unsigned L_M = 0;
    constexpr unsigned L_PART = 8192;
    constexpr unsigned L_P = 16384;
    constexpr unsigned L_RED = 20480;
    constexpr unsigned L_FLAG = 21504;
    const int tid = threadIdx.x, lane = tid & 63;
    const int wave = __builtin_amdgcn_readfirstlane(tid >> 6);
    if (tid == 0) *(LAS unsigned*)(L + L_FLAG + 16) = 0;
    const unsigned parity = load_parity(p.ws);
    const int slot = take_slot((unsigned*)(p.ws + WS_CTRL), (LAS unsigned*)(L + L_FLAG));
    const int sx_ = slot >> 5, st_ = slot & 31;
    const bool isG = st_ >= 28;
    const int sidx = sx_ * 28 + st_;
    const int hs = isG ? sx_ * 4 + (st_ - 28) : sidx / 7, sl = isG ? 0 : sidx % 7;
#define MAPX 0
    const int rg = MAPX == 0 ? (st_ & 3) : MAPX == 1 ? sx_ : (2 * (sx_ & 1) + (st_ & 1));
    const int cg = isG ? 0 : MAPX == 0 ? sx_ * 7 + (st_ >> 2) : MAPX == 1 ? st_ : (sx_ >> 1) * 14 + (st_ >> 1);
    const int rho = lane & 15, q = lane >> 4;
    if (st_ >= 28) {
        unsigned v0 = 0, v1 = 0;
        if (wave == 1) v0 = *(const volatile unsigned*)(ho_base(p.ws) + (size_t)lane * (2u << 20) + (2u << 20) - 64);
        if (wave == 2 && lane < 8) v0 = *(const volatile unsigned*)(ho_base(p.ws) + (size_t)(64 + lane) * (2u << 20) + (2u << 20) - 64);
        if (wave == 3 && lane < 32) v1 = *(const volatile unsigned*)(p.ws + (size_t)lane * (2u << 20) + (1u << 20) - 64);
        asm volatile("" :: "v"(v0), "v"(v1));
    }
    auto rs = __builtin_amdgcn_make_buffer_rsrc(ho_base(p.ws), 0, HO_BYTES, 0x00020000);
    auto rw = __builtin_amdgcn_make_buffer_rsrc(p.ws, 0, 64u << 20, 0x00020000);
    if (slot >= 128) { worker_loop(p, rs, rw, parity, L, (slot >> 5) & 3, slot & 31, wave);
        return; }
    const unsigned l0 = (unsigned)(uintptr_t)smem;
    const int i8 = rho & 7, half = rho >> 3;
    const unsigned mlane = (unsigned)((q >> 1) * 256 + (i8 >> 1) * 64 + (q & 1) * 32 + (i8 & 1) * 16) + half * 512;
    const unsigned mlo = half ? 0u : ~0u, mhi = ~mlo;
    const unsigned nadr = l0 + L_FLAG + 16;
    if (!isG) {
        const int ba = wave < 3 ? (sl + wave + 1) % 7 : sl, bb = wave < 3 ? (sl + 6 - wave) % 7 : sl;
        const int hg = (wave + rg) & 3;
        const int h4 = lane >> 4, i2 = (lane >> 3) & 1, j8 = lane & 7, hc = hg * 4 + h4;
        const unsigned Pw = l0 + L_P + wave * 640;
        const unsigned OOB = 0x3ffffff0u;
        const unsigned osst_ = HO_SMM + (lane >> 4) * 16384 + hs * 512 + sl * 64 + (lane & 3) * 16;
        const unsigned omst_ = HO_M + cg * 256 + rg * 64 + (lane & 3) * 16;
        asm volatile(CHAIN_CONSTS CHAIN_MAIN_ASM
            :
            : [ora] "v"(HO_M + mlane + ba * 1024), [orb] "v"(HO_M + mlane + bb * 1024),
              [osmm] "v"(HO_SMM + rg * 16384 + hc * 512 + i2 * 32 + j8 * 4),
              [osx] "v"(HO_SX + (rg >> 1) * 16384 + (rg & 1) * 4096 + hc * 256 + i2 * 128 + j8 * 16),
              [osst] "v"(((lane & 12) == 0 && wave < 3) ? osst_ + (wave == 2 ? HO_COPY : 0u) : (lane == 4 && wave == 0) ? HO_SMM + (sidx & 3) * 16384 + ((sidx >> 2) & 15) * 512 + 448 + (sidx >> 6) * 16 : OOB),
              [osl0] "v"(HO_SMM + (lane & 3) * 16384 + (lane >> 2) * 512 + 448), [osl1] "v"(lane < 48 ? HO_SMM + (lane & 3) * 16384 + (lane >> 2) * 512 + 464 : OOB),
              [omst] "v"(lane < 4 ? omst_ : OOB), [omra] "v"((lane < 4 && (wave & 1)) ? omst_ + (wave == 3 ? HO_COPY : 0u) : OOB),
              [ou] "v"(R_UF + cg * 32768 + gmap(hg * 4096) + lane * 16),
              [mwa] "v"(l0 + L_M + ba * 1024 + (2 * half + (q >> 1)) * 256 + i8 * 16 + (q & 1) * 8), [mwb] "v"(l0 + L_M + bb * 1024 + (2 * half + (q >> 1)) * 256 + i8 * 16 + (q & 1) * 8),
              [pw] "v"(l0 + L_PART + ((4 * (q & 1)) * 8 + i8) * 80 + (wave * 4 + (q >> 1) * 2 + half) * 4),
              [prd] "v"(l0 + L_PART + ((lane & 48) + (lane & 7) * 2 + ((lane >> 3) & 1)) * 80),
              [osa] "v"(lane == 8 ? HO_SMM + (sidx & 3) * 16384 + ((sidx >> 2) & 15) * 512 + 448 + (sidx >> 6) * 16 : HO_SMM + (lane >> 4) * 16384 + hs * 512 + sl * 64 + (lane & 7) * 8),
              [oma] "v"(HO_M + cg * 256 + rg * 64 + (lane & 7) * 8),
              [mrd] "v"(l0 + L_M + q * 256 + half * 128 + i8 * 16),
              [pwx] "v"(Pw + (1 + h4) * 128 + (j8 >> 1) * 32 + i2 * 16 + (j8 & 1) * 8),
              [pwm] "v"(Pw + (2 * (h4 >> 1) + (j8 >> 2)) * 32 + i2 * 16 + ((h4 & 1) * 4 + (j8 & 3)) * 2),
              [pr] "v"(Pw + q * 32 + (rho & 1) * 16), [prm] "v"(Pw + (q & 1) * 32 + (rho & 1) * 16),
              [redw] "v"(l0 + L_RED + (rho & 1) * 320 + q * 80 + wave * 16), [redr] "v"(wave == 0 ? l0 + L_RED + ((lane >> 1) & 1) * 320 + (((lane >> 2) & 1) * 2 + (lane & 1)) * 80 : l0 + L_RED + (lane & 1) * 320 + ((lane & 2) + ((lane >> 2) & 1)) * 80),
              [mlo] "v"(mlo), [mhi] "v"(mhi), [sidx] "v"(half ? 0xEEEEu : 0x4444u), [nadr] "v"(nadr),
              [oax] "v"((unsigned)WS_WA + ((hs * 28 + 4 * ba) * KK + 2 * bb) * 1024 + lane * 16),
              [oay] "v"((unsigned)WS_WA + ((hs * 28 + 4 * bb) * KK + 2 * ba) * 1024 + lane * 16),
              [ob] "v"((unsigned)WS_WB + ((hg * 4) * 28 + cg) * KK * 1024 + lane * 16),
              [ymask] "v"(wave < 3 ? ~0u : 0u), [owst] "v"(wave == 3 ? (unsigned)WS_MW + (cg * 4 + hg) * 2048 + lane * 16 : OOB), [ofin] "v"((unsigned)WS_CTRL + (CT_M + 16 * 32) * 4), [ocnt] "v"(HO_CNT + (parity & 1u) * 8192u), [opar] "v"(HO_PAR + (parity & 1u) * 8192u), [opst] "v"((sidx == 0 && wave == 3 && lane == 0) ? HO_PAR + (parity & 1u) * 8192u : OOB),
              [rsho] "s"(rs), [rsws] "s"(rw), [par] "s"(parity), [wv] "s"(wave), [ldr] "s"(__builtin_amdgcn_readfirstlane((sidx == 0 && wave == 3) ? 1 : 0))
            : CHAIN_CLOBBERS);
    } else {
        const int b0 = 2 * wave, b1 = wave < 3 ? 2 * wave + 1 : 6;
        asm volatile(CHAIN_CONSTS CHAIN_G_ASM
            :
            : [ora] "v"(HO_M + mlane + b0 * 1024), [orb] "v"(HO_M + mlane + b1 * 1024),
              [oxst] "v"((lane & 12) == 0 ? HO_SX + (wave >> 1) * 16384 + (wave & 1) * 4096 + hs * 256 + (lane >> 4) * 64 + (lane & 3) * 16 : 0x3ffffff0u),
              [og0] "v"(R_GF + hs * 65536 + gmap(b0 * 2048) + lane * 16), [og1] "v"(R_GF + hs * 65536 + gmap(b1 * 2048) + lane * 16),
              [pw] "v"(l0 + L_PART + (wave * 2 + half) * 1024 + i8 * 128 + ((2 * q) ^ i8) * 16), [pw2] "v"(l0 + L_PART + (wave * 2 + half) * 1024 + i8 * 128 + ((2 * q + 1) ^ i8) * 16),
              [prd] "v"(l0 + L_PART + (2 * wave + (lane >> 5)) * 128 + (((((lane >> 4) & 1) * 4 + (lane & 3)) ^ (2 * wave + (lane >> 5))) * 16) + ((lane >> 2) & 3) * 4),
              [mlo] "v"(mlo), [mhi] "v"(mhi), [mlo2] "v"(wave < 3 ? mlo : 0u), [mhi2] "v"(wave < 3 ? mhi : 0u), [nadr] "v"(nadr), [ocnt] "v"(HO_CNT + (parity & 1u) * 8192u), [opar] "v"(HO_PAR + (parity & 1u) * 8192u),
              [rsho] "s"(rs), [rsws] "s"(rw), [par] "s"(parity)
            : CHAIN_CLOBBERS);
    }
}

extern "C" void kernel_launch(void* const* d_in, const int* in_sizes, int n_in,
                              void* d_out, int out_size, void* d_ws, size_t ws_size,
                              hipStream_t stream) {
    Params p;
    p.x = (const u16*)d_in[0]; p.mem0 = (const u16*)d_in[1]; p.wq = (const u16*)d_in[2]; p.wk = (const u16*)d_in[3];
    p.wv = (const u16*)d_in[4]; p.wo = (const u16*)d_in[5];
    p.bx = (const u16*)d_in[7]; p.bwqkv = (const u16*)d_in[8]; p.bwphi = (const u16*)d_in[9]; p.bwo = (const u16*)d_in[10];
    p.out = (u16*)d_out; p.ws = (unsigned char*)d_ws;
    (void)hipFuncSetAttribute((const void*)chain_kernel, hipFuncAttributeMaxDynamicSharedMemorySize, 128 * 1024);
    (void)hipFuncSetAttribute((const void*)prep_kernel, hipFuncAttributeMaxDynamicSharedMemorySize, 120 * 1024);
    prep_kernel<<<256, 512, 120 * 1024, stream>>>(p);
    chain_kernel<<<256, 256, 128 * 1024, stream>>>(p);
}
```
